```python
import torch

def _softmax(x, axis=-1):
    return torch.softmax(x, dim=axis)
def _gelu(x):
    return torch.nn.functional.gelu(x, approximate="tanh")
def _silu(x):
    return torch.nn.functional.silu(x)
def _sigmoid(x):
    return torch.sigmoid(x)
def _layernorm(x, axis=-1, eps=1e-5):
    m = x.mean(dim=axis, keepdim=True)
    v = x.var(dim=axis, keepdim=True, unbiased=False)
    return (x - m) / torch.sqrt(v + eps)
def _scatter_add(tgt, idx, src, axis=0):
    return tgt.clone().index_add_(axis, idx, src)
def _segment_sum(src, seg, n):
    out = torch.zeros((n,) + src.shape[1:], dtype=src.dtype)
    return out.index_add_(0, seg, src)
def _pad(x, pw):
    flat = tuple(v for pair in reversed(pw) for v in pair)
    return torch.nn.functional.pad(x, flat)

kernel_name = 'standalone__hybrid_graph_tf_nagphormer__graph'

def setup_inputs():

    _w = (lambda a: (lambda t: t.to(torch.bfloat16) if t.is_floating_point() else t)(torch.as_tensor(a)))
    N, deg, D = 12500, 32, 128
    E = N * deg
    src = (lambda lo, hi, *s: torch.randint(lo, hi, s))(0, N, E).to(torch.int64)
    dst = (lambda lo, hi, *s: torch.randint(lo, hi, s))(0, N, E).to(torch.int64)
    deg_cnt = torch.bincount(dst, minlength=N).to(float) + 1.0
    sc = 1.0 / D ** 0.5
    return {
        "x": _w((lambda *s: torch.randn(*s, dtype=torch.bfloat16) * 0.1)(N, D)),
        "src": _w(src),
        "dst": _w(dst),
        "deg_inv_sqrt": _w(1.0 / (lambda _x: torch.sqrt(torch.as_tensor(_x)))(deg_cnt)),
        "wq": _w((lambda *s: torch.randn(*s, dtype=torch.bfloat16) * 1.6)(D, D) * sc),
        "wk": _w((lambda *s: torch.randn(*s, dtype=torch.bfloat16) * 1.6)(D, D) * sc),
        "wv": _w((lambda *s: torch.randn(*s, dtype=torch.bfloat16) * 0.1)(D, D) * sc),
        "wo": _w((lambda *s: torch.randn(*s, dtype=torch.bfloat16) * 0.1)(D, D) * sc),
        "w_r": _w((lambda *s: torch.randn(*s, dtype=torch.bfloat16) * 0.1)(D)),
    }

def reference(x, src, dst, deg_inv_sqrt, wq, wk, wv, wo, w_r):
    N, D = x.shape[0], x.shape[1]
    K = 7
    h = x
    toks = [x]
    for _ in range(K):
        msg = h[src] * deg_inv_sqrt[src, None] * deg_inv_sqrt[dst, None]
        h = _segment_sum(msg, dst, N)
        toks.append(h)
    tok = torch.stack(toks, axis=1)
    t = _layernorm(tok, axis=-1)
    q   = torch.einsum('nkd,de->nke', t, wq)
    key = torch.einsum('nkd,de->nke', t, wk)
    v   = torch.einsum('nkd,de->nke', t, wv)
    scale = 1.0 / (D ** 0.5)
    attn = _softmax(torch.einsum('nkd,nld->nkl', q, key) * scale, axis=-1)
    o = torch.einsum('nkl,nld->nkd', attn, v)
    o = tok + torch.einsum('nkd,de->nke', o, wo)
    r = _layernorm(o, axis=-1)
    alpha = _softmax(torch.einsum('nkd,d->nk', r, w_r), axis=-1)
    return torch.einsum('nk,nkd->nd', alpha, o)
```

```cpp
#include <hip/hip_runtime.h>
#include <hip/hip_bf16.h>
#include <stdint.h>

typedef __attribute__((ext_vector_type(2))) __bf16 bf16x2;
typedef __attribute__((ext_vector_type(4))) __bf16 bf16x4;
typedef __attribute__((ext_vector_type(8))) __bf16 bf16x8;
typedef __attribute__((ext_vector_type(4))) float f32x4;
typedef __attribute__((ext_vector_type(16))) float f32x16;
typedef __attribute__((ext_vector_type(2))) unsigned u32x2;
typedef __attribute__((ext_vector_type(4))) unsigned u32x4;
typedef __attribute__((ext_vector_type(6))) unsigned u32x6;
typedef __attribute__((ext_vector_type(8))) unsigned u32x8;
typedef __attribute__((ext_vector_type(16))) unsigned u32x16;
typedef __attribute__((ext_vector_type(32))) unsigned u32x32;
typedef __attribute__((ext_vector_type(4))) int i32x4;
typedef unsigned long long u64;
#define LAS __attribute__((address_space(3)))
#define DI __device__ __forceinline__

#define DLYA 0xb888b0
#define DLYB 0x000000
#define ROT 0
#define POLLS 4
#define POLL0 20
#define ROLE0 1
#define DLY0A 0
#define DLY0B 0
#define CPF 1
#define AP 0
#define MKB 1
#define MB 1
#define DLYC DLYB
#define DLYA1 DLYA
#define ML 2
#define WSL 1
#define MO 1
#define WE 1
#define CW0 8
#define CW1 340
#define CWR 10
#define WSEND 0
#define PFON 1
constexpr int NB = 49;
constexpr int NPAD = 256 * NB;
constexpr int SL = 12560 * 8;
constexpr int HS = 32 * SL;
constexpr int NCH = 128;
constexpr int TS = 40;
constexpr size_t WS_G = 0;
constexpr size_t WS_CELL = (size_t)8 * HS;
constexpr size_t WS_SCHED = WS_CELL + (size_t)256 * NCH * 128;
constexpr size_t WS_META = WS_SCHED + 256 * 5120;
constexpr size_t WS_FLAG = WS_META + 256 * 512;
static_assert((WS_FLAG & 0x7fff) == 0, "flag lines are addressed by masking");
constexpr size_t WS_OCNT = WS_FLAG + 256 * 128;
constexpr size_t WS_RDY = WS_OCNT + 128 * 128;
constexpr size_t WS_EPO = WS_RDY + 128 * 128;
constexpr size_t WS_RAW = WS_EPO + 1024;
constexpr int WSZ = 65536 + 2048;
constexpr size_t WS_W = WS_RAW + 2 * 65536 + 2 * 6144;
constexpr size_t WS_OVF = WS_W + 32 * WSZ;

#define STAMP(K, I)
#define STAMP0(K)
#define FLUSH(K)
DI unsigned pk(float a, float b) { bf16x2 v = {(__bf16)a, (__bf16)b}; return __builtin_bit_cast(unsigned, v); }
DI float blo(unsigned u) { return __uint_as_float(u << 16); }
DI float bhi(unsigned u) { return __uint_as_float(u & 0xffff0000u); }
DI float bf1(unsigned short u) { return __uint_as_float((unsigned)u << 16); }
DI int swap23(int r) { return (r & ~12) | ((r & 4) << 1) | ((r & 8) >> 1); }
template <int C> DI float dppf(float x) { return __int_as_float(__builtin_amdgcn_update_dpp(0, __float_as_int(x), C, 0xf, 0xf, true)); }
DI float wsum(float x) {
  x += dppf<0x128>(x); x += dppf<0x124>(x); x += dppf<0x122>(x); x += dppf<0x121>(x);
  auto a = __builtin_amdgcn_permlane16_swap(__float_as_uint(x), __float_as_uint(x), false, false); x = __uint_as_float(a[0]) + __uint_as_float(a[1]);
  auto c = __builtin_amdgcn_permlane32_swap(__float_as_uint(x), __float_as_uint(x), false, false); return __uint_as_float(c[0]) + __uint_as_float(c[1]);
}
DI void st16(u32x4 v, __amdgpu_buffer_rsrc_t r, unsigned vo, unsigned so) { unsigned o = vo + so; asm("" : "+v"(o)); __builtin_amdgcn_raw_buffer_store_b128(v, r, o, 0, 16); }
#define MFMA32(a, b, c) __builtin_amdgcn_mfma_f32_32x32x16_bf16(__builtin_bit_cast(bf16x8, a), __builtin_bit_cast(bf16x8, b), (c), 0, 0, 0)
#define MFMA16(a, b, c) __builtin_amdgcn_mfma_f32_16x16x32_bf16(__builtin_bit_cast(bf16x8, a), __builtin_bit_cast(bf16x8, b), (c), 0, 0, 0)

template <int m> DI void wprod(u32x4 va, u32x4 vb, const uint16_t* __restrict__ wr, __amdgpu_buffer_rsrc_t rsrc, char* sm, int qd) {
  const int t = threadIdx.x, w = t >> 6, l = t & 63, r = l & 31, h = l >> 5, lr = (w >> 1) & 1, lc = w & 1, tr = (qd >> 1) * 2 + lr, tc = (qd & 1) * 2 + lc;
  char* As = sm; char* Bs = As + 64 * 272;
  u32x4 wv[2];
  if (m == 1) { wv[0] = *(const u32x4*)(wr + 32 * tc + 8 * h); wv[1] = *(const u32x4*)(wr + 32 * tc + 16 + 8 * h); asm volatile("" : "+v"(wv[0]), "+v"(wv[1])); }
  *(u32x4*)(As + (t >> 4) * 272 + (t & 15) * 16) = va;
  *(u32x4*)(Bs + (m == 0 ? (t >> 4) * 272 + (t & 15) * 16 : (t >> 3) * 144 + (t & 7) * 16)) = vb;
  __syncthreads();
  if (w >= 8) return;
  const int pr = swap23(r);
  u32x4 a[8], bb[8];
#pragma unroll
  for (int s = 0; s < 8; ++s) {
    a[s] = *(const u32x4*)(As + (lr * 32 + pr) * 272 + s * 32 + h * 16);
    if (m == 0) bb[s] = *(const u32x4*)(Bs + (lc * 32 + pr) * 272 + s * 32 + h * 16);
    else { const int q = (l >> 2) & 3, p = l & 3, ps = ((p & 1) << 1) | (p >> 1);
      const char* ad = Bs + (16 * s + 8 * h + q) * 144 + (lc * 32 + (l & 16) + 4 * ps) * 2;
      const bf16x4 lo = __builtin_amdgcn_ds_read_tr16_b64_v4bf16((LAS bf16x4*)ad), hi = __builtin_amdgcn_ds_read_tr16_b64_v4bf16((LAS bf16x4*)(ad + 4 * 144));
      const u32x2 ul = __builtin_bit_cast(u32x2, lo), uh = __builtin_bit_cast(u32x2, hi); bb[s] = u32x4{ul[0], ul[1], uh[0], uh[1]}; }
  }
  const bool tp = __builtin_amdgcn_readfirstlane(w) >= 4;
  f32x16 acc; for (int i = 0; i < 16; ++i) acc[i] = 0.f;
#pragma unroll
  for (int s = 0; s < 8; ++s) acc = MFMA32(tp ? bb[s] : a[s], tp ? a[s] : bb[s], acc);
  float c = 0.f, dw = 0.f;
#pragma unroll
  for (int i = 0; i < 16; ++i) c += acc[i];
  if (m == 1) {
#pragma unroll
    for (int i = 0; i < 8; ++i) { const unsigned u = wv[i >> 2][i & 3]; dw += acc[2 * i] * blo(u) + acc[2 * i + 1] * bhi(u); } }
  auto x = __builtin_amdgcn_permlane32_swap(__float_as_uint(c), __float_as_uint(dw), false, false);
  if (tp || !h) __builtin_amdgcn_raw_buffer_store_b32(__float_as_uint(__uint_as_float(x[0]) + __uint_as_float(x[1])), rsrc,
                    (unsigned)WS_RAW + 2 * 65536 + m * 6144 + (tp ? tc * 384 + 128 + 128 * h + 32 * tr + pr : tr * 384 + 32 * tc + pr) * 4, 0, 16);
  if (!tp) {
#pragma unroll
    for (int i = 0; i < 4; ++i) { const f32x4 o = {acc[4 * i], acc[4 * i + 1], acc[4 * i + 2], acc[4 * i + 3]}; st16(__builtin_bit_cast(u32x4, o), rsrc, (tc * 4 + tr) * 4096 + i * 1024 + l * 16, (unsigned)WS_RAW + m * 65536); } }
}
DI float sum4(f32x4 v) { return (v[0] + v[1]) + (v[2] + v[3]); }
DI f32x4 ld16(__amdgpu_buffer_rsrc_t r, unsigned o) { const u32x4 v = __builtin_amdgcn_raw_buffer_load_b128(r, o, 0, 16); return __builtin_bit_cast(f32x4, v); }
DI f32x4 ld4(__amdgpu_buffer_rsrc_t r, unsigned o) { f32x4 v;
#pragma unroll
  for (int i = 0; i < 4; ++i) v[i] = __uint_as_float(__builtin_amdgcn_raw_buffer_load_b32(r, o + i * 1536, 0, 16));
  return v; }
DI void wfetch(f32x4* v, __amdgpu_buffer_rsrc_t r, int k, int l) {
  const unsigned part = (unsigned)WS_RAW + 2 * 65536 + (k >> 4) * 6144;
#pragma unroll
  for (int i = 0; i < 4; ++i) v[i] = ld16(r, (unsigned)WS_RAW + k * 4096 + i * 1024 + l * 16);
  v[4] = ld4(r, part + (l < 32 ? 32 * ((k >> 2) & 3) + swap23(l) : 96 + 32 * (k & 3) + l) * 4);
  v[5] = ld16(r, part + (l >> 5) * 1536 + (l & 31) * 16); v[6] = ld16(r, part + (2 + (l >> 5)) * 1536 + (l & 31) * 16);
}
DI void wfinish(f32x4* v, __amdgpu_buffer_rsrc_t rsrc, unsigned wb, int k, int l, unsigned scr) {
  const float rf = (k >> 4) ? 0.f : 1.f / 128;
  const unsigned s4 = __float_as_uint(sum4(v[4])); const auto sw = __builtin_amdgcn_permlane32_swap(s4, s4, false, false);
  *(LAS float*)(size_t)(scr + (l & 31) * 4) = __uint_as_float(sw[1]) * rf;
  asm volatile("" ::: "memory");
  const float sub = __uint_as_float(sw[0]) * (1.f / 128) - wsum(sum4(v[5]) + sum4(v[6])) * rf * (1.f / 128);
#pragma unroll
  for (int i = 0; i < 4; ++i) v[i] -= *(LAS f32x4*)(size_t)(scr + (16 * (i >> 1) + 8 * (l >> 5) + 4 * (i & 1)) * 4) + sub;
#pragma unroll
  for (int s = 0; s < 2; ++s) st16(u32x4{pk(v[2 * s][0], v[2 * s][1]), pk(v[2 * s][2], v[2 * s][3]), pk(v[2 * s + 1][0], v[2 * s + 1][1]), pk(v[2 * s + 1][2], v[2 * s + 1][3])},
                                   rsrc, l * 16, wb + k * 2048 + s * 1024);
}
DI void ffetch(f32x4* v, __amdgpu_buffer_rsrc_t r, const uint16_t* __restrict__ wr, int l) {
#pragma unroll
  for (int i = 0; i < 6; ++i) v[i] = ld4(r, (unsigned)WS_RAW + 2 * 65536 + 6144 + (2 * l + (i >> 1) * 128 + (i & 1)) * 4);
  v[6][0] = __uint_as_float(*(const unsigned*)(wr + 2 * l));
}
DI void ffinish(const f32x4* v, __amdgpu_buffer_rsrc_t rsrc, unsigned wb, int l) {
  const unsigned u = __float_as_uint(v[6][0]); const float c0 = sum4(v[0]), c1 = sum4(v[1]);
  const float tot = wsum(c0 + c1) * (1.f / 128), cw = wsum(c0 * blo(u) + c1 * bhi(u)) * (1.f / 128), sw = wsum(blo(u) + bhi(u));
  const unsigned o[6] = {0x3f803f80u, u, pk(sum4(v[4]) - cw, sum4(v[5]) - cw), pk(sum4(v[2]) - tot, sum4(v[3]) - tot), 0u, __float_as_uint(sw)};
#pragma unroll
  for (int i = 0; i < 6; ++i) __builtin_amdgcn_raw_buffer_store_b32(o[i], rsrc, wb + 65536 + i * 256 + l * 4, 0, 16);
}

constexpr int S_COL = 512, S_DEG = 1024, S_CNT = 1792, S_S = 2080, S_LEFT = S_S + 5120, S_FULL = S_LEFT + 5120 * 4, S_EXTRA = S_FULL + 512, S_END = S_EXTRA + 4096;
#define LW(a) ((LAS unsigned*)(size_t)(a))
#define WG_ __ATOMIC_RELAXED, __HIP_MEMORY_SCOPE_WORKGROUP
DI unsigned l_add(unsigned a, unsigned v) { return __hip_atomic_fetch_add(LW(a), v, WG_); }
DI void lspin(unsigned a, unsigned n) { while ((unsigned)__builtin_amdgcn_readfirstlane(__hip_atomic_load(LW(a), WG_)) < n) __builtin_amdgcn_s_sleep(1); asm volatile("" ::: "memory"); }
DI int mbc(u64 m) { return __builtin_amdgcn_mbcnt_hi((unsigned)(m >> 32), __builtin_amdgcn_mbcnt_lo((unsigned)m, 0)); }
#define PASS1(R, NA, M, T, E) asm volatile( \
  "v_mov_b32 %9, 1\n\t" \
  "v_cmp_ne_u32 %6, -1, %10\n\t" \
  "v_bfe_u32 %3, %10, 14, 8\n\t" \
  "v_cmp_ne_u32 %7, -1, %11\n\t" \
  "v_bfe_u32 %4, %11, 14, 8\n\t" \
  "v_cmp_ne_u32 %8, -1, %12\n\t" \
  "v_bfe_u32 %5, %12, 14, 8\n\t" \
  "s_mov_b64 exec, %6\n\t" \
  "ds_add_rtn_u32 %0, %3, %9 offset:1024\n\t" \
  "s_mov_b64 exec, %7\n\t" \
  "ds_add_rtn_u32 %1, %4, %9 offset:1024\n\t" \
  "s_mov_b64 exec, %8\n\t" \
  "ds_add_rtn_u32 %2, %5, %9 offset:1024\n\t" \
  "s_mov_b64 exec, -1\n\t" \
  : "=&v"(R[0]), "=&v"(R[1]), "=&v"(R[2]), "=&v"(NA[0]), "=&v"(NA[1]), "=&v"(NA[2]), "=&s"(M[0]), "=&s"(M[1]), "=&s"(M[2]), "=&v"(T) \
  : "v"(E[0]), "v"(E[1]), "v"(E[2]) \
  : "memory")
#define PASS2(E, R, NA, T, LM, ST, M, INFO, L4) asm volatile( \
  "ds_write_b32 %23, %22 offset:1280\n\t" \
  "s_mov_b64 exec, %19\n\t" \
  "ds_read_b32 %6, %6 offset:1280\n\t" \
  "s_mov_b64 exec, %20\n\t" \
  "ds_read_b32 %7, %7 offset:1280\n\t" \
  "s_mov_b64 exec, %21\n\t" \
  "ds_read_b32 %8, %8 offset:1280\n\t" \
  "s_mov_b64 exec, %19\n\t" \
  "s_waitcnt lgkmcnt(2)\n\t" \
  "v_bitop3_b32 %3, %6, %3, 1 bitop3:0x80\n\t" \
  "v_and_b32 %6, -4, %6\n\t" \
  "v_lshl_add_u32 %6, %3, 2, %6\n\t" \
  "v_lshrrev_b32 %3, 2, %6\n\t" \
  "v_sub_u32 %13, %0, %3\n\t" \
  "v_lshlrev_b32 %9, %13, 1\n\t" \
  "ds_or_rtn_b32 %11, %6, %9 offset:0\n\t" \
  "v_bfi_b32 %3, 31, %0, %3\n\t" \
  "v_lshlrev_b32 %3, 2, %3\n\t" \
  "v_mov_b32_sdwa %0, %6 dst_sel:WORD_1 dst_unused:UNUSED_PRESERVE src0_sel:WORD_0\n\t" \
  "v_and_b32 %13, 31, %13\n\t" \
  "v_mul_u32_u24 %6, 20, %6\n\t" \
  "v_lshl_add_u32 %6, %13, 1, %6\n\t" \
  "s_mov_b64 exec, %20\n\t" \
  "s_waitcnt lgkmcnt(2)\n\t" \
  "v_bitop3_b32 %4, %7, %4, 1 bitop3:0x80\n\t" \
  "v_and_b32 %7, -4, %7\n\t" \
  "v_lshl_add_u32 %7, %4, 2, %7\n\t" \
  "v_lshrrev_b32 %4, 2, %7\n\t" \
  "v_sub_u32 %13, %1, %4\n\t" \
  "v_lshlrev_b32 %10, %13, 1\n\t" \
  "ds_or_rtn_b32 %12, %7, %10 offset:0\n\t" \
  "v_bfi_b32 %4, 31, %1, %4\n\t" \
  "v_lshlrev_b32 %4, 2, %4\n\t" \
  "v_mov_b32_sdwa %1, %7 dst_sel:WORD_1 dst_unused:UNUSED_PRESERVE src0_sel:WORD_0\n\t" \
  "v_and_b32 %13, 31, %13\n\t" \
  "v_mul_u32_u24 %7, 20, %7\n\t" \
  "v_lshl_add_u32 %7, %13, 1, %7\n\t" \
  "s_mov_b64 exec, %21\n\t" \
  "s_waitcnt lgkmcnt(2)\n\t" \
  "v_bitop3_b32 %5, %8, %5, 1 bitop3:0x80\n\t" \
  "v_and_b32 %8, -4, %8\n\t" \
  "v_lshl_add_u32 %8, %5, 2, %8\n\t" \
  "v_mov_b32_sdwa %2, %8 dst_sel:WORD_1 dst_unused:UNUSED_PRESERVE src0_sel:WORD_0\n\t" \
  "s_bcnt1_i32_b64 %17, %21\n\t" \
  "s_waitcnt lgkmcnt(0)\n\t" \
  "s_mov_b64 exec, %19\n\t" \
  "v_and_b32 %11, %11, %9\n\t" \
  "v_cmp_ne_u32 %15, 0, %11\n\t" \
  "s_mov_b64 exec, %20\n\t" \
  "v_and_b32 %12, %12, %10\n\t" \
  "v_cmp_ne_u32 %16, 0, %12\n\t" \
  "s_bcnt1_i32_b64 %18, %15\n\t" \
  "s_add_u32 %17, %17, %18\n\t" \
  "s_bcnt1_i32_b64 %18, %16\n\t" \
  "s_add_u32 %17, %17, %18\n\t" \
  "s_mov_b64 exec, 1\n\t" \
  "v_mov_b32 %13, %17\n\t" \
  "v_mov_b32 %14, 0\n\t" \
  "ds_add_rtn_u32 %14, %14, %13 offset:1792\n\t" \
  "s_andn2_b64 exec, %19, %15\n\t" \
  "ds_or_b32 %3, %9 offset:512\n\t" \
  "ds_write_b16 %6, %0 offset:2080\n\t" \
  "s_andn2_b64 exec, %20, %16\n\t" \
  "ds_or_b32 %4, %10 offset:512\n\t" \
  "ds_write_b16 %7, %1 offset:2080\n\t" \
  "s_mov_b64 exec, -1\n\t" \
  "s_mov_b64 vcc, %15\n\t" \
  "v_mbcnt_lo_u32_b32 %11, vcc_lo, 0\n\t" \
  "s_bcnt1_i32_b64 %17, vcc\n\t" \
  "v_mbcnt_hi_u32_b32 %11, vcc_hi, %11\n\t" \
  "s_mov_b64 vcc, %16\n\t" \
  "v_mov_b32 %12, %17\n\t" \
  "v_mbcnt_lo_u32_b32 %12, vcc_lo, %12\n\t" \
  "s_bcnt1_i32_b64 %18, vcc\n\t" \
  "s_add_u32 %17, %17, %18\n\t" \
  "v_mbcnt_hi_u32_b32 %12, vcc_hi, %12\n\t" \
  "s_mov_b64 vcc, %21\n\t" \
  "v_mov_b32 %13, %17\n\t" \
  "v_mbcnt_lo_u32_b32 %13, vcc_lo, %13\n\t" \
  "v_mbcnt_hi_u32_b32 %13, vcc_hi, %13\n\t" \
  "s_waitcnt lgkmcnt(4)\n\t" \
  "v_readfirstlane_b32 %17, %14\n\t" \
  "s_nop 1\n\t" \
  "s_mov_b64 exec, %15\n\t" \
  "v_add_lshl_u32 %11, %11, %17, 2\n\t" \
  "ds_write_b32 %11, %0 offset:7200\n\t" \
  "s_mov_b64 exec, %16\n\t" \
  "v_add_lshl_u32 %12, %12, %17, 2\n\t" \
  "ds_write_b32 %12, %1 offset:7200\n\t" \
  "s_mov_b64 exec, %21\n\t" \
  "v_add_lshl_u32 %13, %13, %17, 2\n\t" \
  "ds_write_b32 %13, %2 offset:7200\n\t" \
  "s_mov_b64 exec, -1\n\t" \
  : "+v"(E[0]), "+v"(E[1]), "+v"(E[2]), "+v"(R[0]), "+v"(R[1]), "+v"(R[2]), "+v"(NA[0]), "+v"(NA[1]), "+v"(NA[2]), "=&v"(T[0]), "=&v"(T[1]), "=&v"(T[2]), "=&v"(T[3]), "=&v"(T[4]), "=&v"(T[5]), "=&s"(LM[0]), "=&s"(LM[1]), "=&s"(ST[0]), "=&s"(ST[1]) \
  : "s"(M[0]), "s"(M[1]), "s"(M[2]), "v"(INFO), "v"(L4) \
  : "memory", "scc", "vcc")
#define PASS3(TD, AL, TX, V, HH) asm volatile( \
  "v_cmp_ne_u32 %1, -1, %3\n\t" \
  "v_lshrrev_b32 v100, 16, %3\n\t" \
  "v_lshlrev_b32 v101, 2, %3\n\t" \
  "v_mov_b32 v103, 0x7c\n\t" \
  "s_mov_b64 exec, %1\n\t" \
  "s_cbranch_execz .Lz_%=\n\t" \
  "v_bfi_b32 v101, v103, v101, v100\n\t" \
  "v_add_u32 v101, 512, v101\n\t" \
  "ds_read_b32 v104, v100 offset:0\n\t" \
  "ds_read_b32 v106, v101 offset:0\n\t" \
  "v_mov_b32 v108, %4\n\t" \
  "v_mov_b32 v111, 0xff\n\t" \
  "v_mov_b32 v105, 0\n\t" \
  "v_mov_b32 v107, 0\n\t" \
  "s_mov_b64 %0, exec\n\t" \
  "s_waitcnt lgkmcnt(0)\n\t" \
  "v_bitop3_b32 v103, v104, v106, 0 bitop3:0x03\n\t" \
  "v_cmpx_ne_u32 vcc, 0, v103\n\t" \
  "v_alignbit_b32 v103, v103, v103, v108\n\t" \
  "v_ffbl_b32 v103, v103\n\t" \
  "v_add_u32 v103, v103, v108\n\t" \
  "v_and_b32 v102, 31, v103\n\t" \
  "v_lshlrev_b32 v109, v102, 1\n\t" \
  "ds_or_rtn_b32 v104, v100, v109 offset:0\n\t" \
  "ds_or_rtn_b32 v106, v101, v109 offset:0\n\t" \
  "v_lshrrev_b32 v108, 6, v108\n\t" \
  "s_waitcnt lgkmcnt(0)\n\t" \
  "v_bitop3_b32 v103, v104, v106, v109 bitop3:0xa8\n\t" \
  "v_and_b32 v110, v104, v109\n\t" \
  "v_cmp_eq_u32 vcc, 0, v103\n\t" \
  "v_bitop3_b32 v103, v104, v106, v109 bitop3:0x28\n\t" \
  "s_andn2_b64 %0, %0, vcc\n\t" \
  "v_cmp_eq_u32 vcc, 0, v110\n\t" \
  "v_cmpx_ne_u32 %2, 0, v103\n\t" \
  "s_nop 0\n\t" \
  "v_cndmask_b32 v110, v101, v100, vcc\n\t" \
  "ds_xor_b32 v110, v109 offset:0\n\t" \
  "s_mov_b64 exec, %0\n\t" \
  "s_cbranch_execz .Lw_%=\n\t" \
  "v_bitop3_b32 v103, v105, v107, v111 bitop3:0x02\n\t" \
  "v_perm_b32 v103, v103, v103, 0\n\t" \
  "v_cmpx_ne_u32 vcc, 0, v103\n\t" \
  "v_alignbit_b32 v103, v103, v103, v108\n\t" \
  "v_ffbl_b32 v103, v103\n\t" \
  "v_add_u32 v103, v103, v108\n\t" \
  "v_and_or_b32 v102, v103, 7, 32\n\t" \
  "v_lshlrev_b32 v109, v102, 1\n\t" \
  "ds_or_rtn_b32 v105, v100, v109 offset:256\n\t" \
  "ds_or_rtn_b32 v107, v101, v109 offset:256\n\t" \
  "ds_read_b32 v104, v100 offset:0\n\t" \
  "ds_read_b32 v106, v101 offset:0\n\t" \
  "v_lshrrev_b32 v108, 6, v108\n\t" \
  "s_waitcnt lgkmcnt(0)\n\t" \
  "v_bitop3_b32 v103, v105, v107, v109 bitop3:0xa8\n\t" \
  "v_and_b32 v110, v105, v109\n\t" \
  "v_cmp_eq_u32 vcc, 0, v103\n\t" \
  "v_bitop3_b32 v103, v105, v107, v109 bitop3:0x28\n\t" \
  "s_andn2_b64 %0, %0, vcc\n\t" \
  "v_cmp_eq_u32 vcc, 0, v110\n\t" \
  "v_cmpx_ne_u32 %2, 0, v103\n\t" \
  "s_nop 0\n\t" \
  "v_cndmask_b32 v110, v101, v100, vcc\n\t" \
  "ds_xor_b32 v110, v109 offset:256\n\t" \
  "s_mov_b64 exec, %0\n\t" \
  "s_cbranch_execz .Lw_%=\n\t" \
  "v_bitop3_b32 v103, v104, v106, 0 bitop3:0x03\n\t" \
  "v_cmpx_ne_u32 vcc, 0, v103\n\t" \
  "v_alignbit_b32 v103, v103, v103, v108\n\t" \
  "v_ffbl_b32 v103, v103\n\t" \
  "v_add_u32 v103, v103, v108\n\t" \
  "v_and_b32 v102, 31, v103\n\t" \
  "v_lshlrev_b32 v109, v102, 1\n\t" \
  "ds_or_rtn_b32 v104, v100, v109 offset:0\n\t" \
  "ds_or_rtn_b32 v106, v101, v109 offset:0\n\t" \
  "ds_read_b32 v105, v100 offset:256\n\t" \
  "ds_read_b32 v107, v101 offset:256\n\t" \
  "v_lshrrev_b32 v108, 6, v108\n\t" \
  "s_waitcnt lgkmcnt(0)\n\t" \
  "v_bitop3_b32 v103, v104, v106, v109 bitop3:0xa8\n\t" \
  "v_and_b32 v110, v104, v109\n\t" \
  "v_cmp_eq_u32 vcc, 0, v103\n\t" \
  "v_bitop3_b32 v103, v104, v106, v109 bitop3:0x28\n\t" \
  "s_andn2_b64 %0, %0, vcc\n\t" \
  "v_cmp_eq_u32 vcc, 0, v110\n\t" \
  "v_cmpx_ne_u32 %2, 0, v103\n\t" \
  "s_nop 0\n\t" \
  "v_cndmask_b32 v110, v101, v100, vcc\n\t" \
  "ds_xor_b32 v110, v109 offset:0\n\t" \
  "s_mov_b64 exec, %0\n\t" \
  "s_cbranch_execz .Lw_%=\n\t" \
  "v_bitop3_b32 v103, v105, v107, v111 bitop3:0x02\n\t" \
  "v_perm_b32 v103, v103, v103, 0\n\t" \
  "v_cmpx_ne_u32 vcc, 0, v103\n\t" \
  "v_alignbit_b32 v103, v103, v103, v108\n\t" \
  "v_ffbl_b32 v103, v103\n\t" \
  "v_add_u32 v103, v103, v108\n\t" \
  "v_and_or_b32 v102, v103, 7, 32\n\t" \
  "v_lshlrev_b32 v109, v102, 1\n\t" \
  "ds_or_rtn_b32 v105, v100, v109 offset:256\n\t" \
  "ds_or_rtn_b32 v107, v101, v109 offset:256\n\t" \
  "ds_read_b32 v104, v100 offset:0\n\t" \
  "v_lshrrev_b32 v108, 6, v108\n\t" \
  "s_waitcnt lgkmcnt(0)\n\t" \
  "v_bitop3_b32 v103, v105, v107, v109 bitop3:0xa8\n\t" \
  "v_and_b32 v110, v105, v109\n\t" \
  "v_cmp_eq_u32 vcc, 0, v103\n\t" \
  "v_bitop3_b32 v103, v105, v107, v109 bitop3:0x28\n\t" \
  "s_andn2_b64 %0, %0, vcc\n\t" \
  "v_cmp_eq_u32 vcc, 0, v110\n\t" \
  "v_cmpx_ne_u32 %2, 0, v103\n\t" \
  "s_nop 0\n\t" \
  "v_cndmask_b32 v110, v101, v100, vcc\n\t" \
  "ds_xor_b32 v110, v109 offset:256\n\t" \
  "s_mov_b64 exec, %0\n\t" \
  "s_cbranch_execz .Lw_%=\n\t" \
  ".Lf_%=:\n\t" \
  "v_bitop3_b32 v103, v105, v105, v111 bitop3:0x02\n\t" \
  "v_not_b32 v110, v104\n\t" \
  "v_ffbh_u32 v103, v103\n\t" \
  "v_ffbh_u32 v110, v110\n\t" \
  "v_add_u32 v110, 32, v110\n\t" \
  "v_min_u32 v103, v103, v110\n\t" \
  "v_sub_u32 v102, 63, v103\n\t" \
  "v_lshlrev_b32 v109, v102, 1\n\t" \
  "v_and_b32 v110, 32, v102\n\t" \
  "v_lshl_add_u32 v103, v110, 3, v100\n\t" \
  "v_lshl_add_u32 v110, v110, 3, v101\n\t" \
  "ds_or_rtn_b32 v103, v103, v109\n\t" \
  "ds_read_b32 v104, v100 offset:0\n\t" \
  "ds_read_b32 v105, v100 offset:256\n\t" \
  "s_waitcnt lgkmcnt(0)\n\t" \
  "v_and_b32 v103, v103, v109\n\t" \
  "v_cmp_eq_u32 vcc, 0, v103\n\t" \
  "s_andn2_b64 %0, %0, vcc\n\t" \
  "s_mov_b64 exec, vcc\n\t" \
  "ds_or_b32 v110, v109\n\t" \
  "s_mov_b64 exec, %0\n\t" \
  "s_cbranch_execnz .Lf_%=\n\t" \
  ".Lw_%=:\n\t" \
  "s_mov_b64 exec, %1\n\t" \
  "v_mul_u32_u24 v100, 20, v100\n\t" \
  "v_lshl_add_u32 v100, v102, 1, v100\n\t" \
  "ds_write_b16 v100, %3 offset:2080\n\t" \
  ".Lz_%=:\n\t" \
  "s_mov_b64 exec, -1\n\t" \
  : "=&s"(TD), "=&s"(AL), "=&s"(TX) \
  : "v"(V), "v"(HH) \
  : "memory", "scc", "vcc", "v100", "v101", "v102", "v103", "v104", "v105", "v106", "v107", "v108", "v109", "v110", "v111")

#define HOP_LOOP(SOFF, HOP, TMP, RETRY, MK0, MK1, ROLE, DCUR, RSRC, GRP, DLY, HS, M49, SO, MO, SB, FP, MS, FO, FH, FL, PH, PM, MT, BT, M8, O6, L16, PO, MO2) asm volatile( \
  "v_lshrrev_b32 v25, 1, v26\n\t" \
  "v_mov_b32 v22, 153520\n\t" \
  ".Lrd_%=:\n\t" \
  "ds_read_b32 v23, v22 offset:16\n\t" \
  "ds_read_b32 v24, v22 offset:32\n\t" \
  "s_waitcnt lgkmcnt(0)\n\t" \
  "v_cmp_ne_u32 vcc, 0, v24\n\t" \
  "s_cbranch_vccnz .Lslow_%=\n\t" \
  "v_cmp_eq_u32 vcc, -1, v23\n\t" \
  "s_cbranch_vccnz .Lsc_%=\n\t" \
  "s_sleep 2\n\t" \
  "s_branch .Lrd_%=\n\t" \
  ".Lsc_%=:\n\t" \
  "buffer_load_dwordx2 v[2:3], v25, %10, %16 offen sc1\n\t" \
  "buffer_load_dwordx2 v[4:5], v25, %10, %16 offen offset:512 sc1\n\t" \
  "s_mov_b32 %2, %15\n\t" \
  "buffer_load_dwordx4 v[36:39], v26, %10, %2 offen offset:0 sc1\n\t" \
  "buffer_load_dwordx4 v[44:47], v26, %10, %2 offen offset:1024 sc1\n\t" \
  "buffer_load_dwordx4 v[52:55], v26, %10, %2 offen offset:2048 sc1\n\t" \
  "buffer_load_dwordx4 v[60:63], v26, %10, %2 offen offset:3072 sc1\n\t" \
  "s_add_u32 %2, %2, 4096\n\t" \
  "buffer_load_dwordx4 v[68:71], v26, %10, %2 offen offset:0 sc1\n\t" \
  "buffer_load_dwordx4 v[76:79], v26, %10, %2 offen offset:1024 sc1\n\t" \
  "buffer_load_dwordx4 v[84:87], v26, %10, %2 offen offset:2048 sc1\n\t" \
  "buffer_load_dwordx4 v[92:95], v26, %10, %2 offen offset:3072 sc1\n\t" \
  "s_add_u32 %2, %2, 4096\n\t" \
  "buffer_load_dwordx4 v[100:103], v26, %10, %2 offen offset:0 sc1\n\t" \
  "buffer_load_dwordx4 v[108:111], v26, %10, %2 offen offset:1024 sc1\n\t" \
  "s_waitcnt vmcnt(10)\n\t" \
  "v_max3_u32 v24, v2, v3, v4\n\t" \
  "v_max_u32 v24, v24, v5\n\t" \
  "v_cmp_gt_i32 %4, 0, v2\n\t" \
  "v_cmp_gt_i32 %5, 0, v4\n\t" \
  "v_and_b32 v2, 0xffff, v2\n\t" \
  "v_and_b32 v4, 0xffff, v4\n\t" \
  "v_add_u32 v2, %17, v2\n\t" \
  "v_add_u32 v4, %17, v4\n\t" \
  "s_waitcnt vmcnt(9)\n\t" \
  "v_max3_u32 v24, v24, v36, v37\n\t" \
  "v_max3_u32 v24, v24, v38, v39\n\t" \
  "v_lshlrev_b32_sdwa v32, 3, v36 dst_sel:DWORD dst_unused:UNUSED_PAD src0_sel:DWORD src1_sel:WORD_0\n\t" \
  "v_lshlrev_b32_sdwa v33, 3, v36 dst_sel:DWORD dst_unused:UNUSED_PAD src0_sel:DWORD src1_sel:WORD_1\n\t" \
  "v_lshlrev_b32_sdwa v34, 3, v37 dst_sel:DWORD dst_unused:UNUSED_PAD src0_sel:DWORD src1_sel:WORD_0\n\t" \
  "v_lshlrev_b32_sdwa v35, 3, v37 dst_sel:DWORD dst_unused:UNUSED_PAD src0_sel:DWORD src1_sel:WORD_1\n\t" \
  "v_lshlrev_b32_sdwa v36, 3, v38 dst_sel:DWORD dst_unused:UNUSED_PAD src0_sel:DWORD src1_sel:WORD_0\n\t" \
  "v_lshlrev_b32_sdwa v37, 3, v38 dst_sel:DWORD dst_unused:UNUSED_PAD src0_sel:DWORD src1_sel:WORD_1\n\t" \
  "v_lshlrev_b32_sdwa v38, 3, v39 dst_sel:DWORD dst_unused:UNUSED_PAD src0_sel:DWORD src1_sel:WORD_0\n\t" \
  "v_lshlrev_b32_sdwa v39, 3, v39 dst_sel:DWORD dst_unused:UNUSED_PAD src0_sel:DWORD src1_sel:WORD_1\n\t" \
  "s_waitcnt vmcnt(8)\n\t" \
  "v_max3_u32 v24, v24, v44, v45\n\t" \
  "v_max3_u32 v24, v24, v46, v47\n\t" \
  "v_lshlrev_b32_sdwa v40, 3, v44 dst_sel:DWORD dst_unused:UNUSED_PAD src0_sel:DWORD src1_sel:WORD_0\n\t" \
  "v_lshlrev_b32_sdwa v41, 3, v44 dst_sel:DWORD dst_unused:UNUSED_PAD src0_sel:DWORD src1_sel:WORD_1\n\t" \
  "v_lshlrev_b32_sdwa v42, 3, v45 dst_sel:DWORD dst_unused:UNUSED_PAD src0_sel:DWORD src1_sel:WORD_0\n\t" \
  "v_lshlrev_b32_sdwa v43, 3, v45 dst_sel:DWORD dst_unused:UNUSED_PAD src0_sel:DWORD src1_sel:WORD_1\n\t" \
  "v_lshlrev_b32_sdwa v44, 3, v46 dst_sel:DWORD dst_unused:UNUSED_PAD src0_sel:DWORD src1_sel:WORD_0\n\t" \
  "v_lshlrev_b32_sdwa v45, 3, v46 dst_sel:DWORD dst_unused:UNUSED_PAD src0_sel:DWORD src1_sel:WORD_1\n\t" \
  "v_lshlrev_b32_sdwa v46, 3, v47 dst_sel:DWORD dst_unused:UNUSED_PAD src0_sel:DWORD src1_sel:WORD_0\n\t" \
  "v_lshlrev_b32_sdwa v47, 3, v47 dst_sel:DWORD dst_unused:UNUSED_PAD src0_sel:DWORD src1_sel:WORD_1\n\t" \
  "s_waitcnt vmcnt(7)\n\t" \
  "v_max3_u32 v24, v24, v52, v53\n\t" \
  "v_max3_u32 v24, v24, v54, v55\n\t" \
  "v_lshlrev_b32_sdwa v48, 3, v52 dst_sel:DWORD dst_unused:UNUSED_PAD src0_sel:DWORD src1_sel:WORD_0\n\t" \
  "v_lshlrev_b32_sdwa v49, 3, v52 dst_sel:DWORD dst_unused:UNUSED_PAD src0_sel:DWORD src1_sel:WORD_1\n\t" \
  "v_lshlrev_b32_sdwa v50, 3, v53 dst_sel:DWORD dst_unused:UNUSED_PAD src0_sel:DWORD src1_sel:WORD_0\n\t" \
  "v_lshlrev_b32_sdwa v51, 3, v53 dst_sel:DWORD dst_unused:UNUSED_PAD src0_sel:DWORD src1_sel:WORD_1\n\t" \
  "v_lshlrev_b32_sdwa v52, 3, v54 dst_sel:DWORD dst_unused:UNUSED_PAD src0_sel:DWORD src1_sel:WORD_0\n\t" \
  "v_lshlrev_b32_sdwa v53, 3, v54 dst_sel:DWORD dst_unused:UNUSED_PAD src0_sel:DWORD src1_sel:WORD_1\n\t" \
  "v_lshlrev_b32_sdwa v54, 3, v55 dst_sel:DWORD dst_unused:UNUSED_PAD src0_sel:DWORD src1_sel:WORD_0\n\t" \
  "v_lshlrev_b32_sdwa v55, 3, v55 dst_sel:DWORD dst_unused:UNUSED_PAD src0_sel:DWORD src1_sel:WORD_1\n\t" \
  "s_waitcnt vmcnt(6)\n\t" \
  "v_max3_u32 v24, v24, v60, v61\n\t" \
  "v_max3_u32 v24, v24, v62, v63\n\t" \
  "v_lshlrev_b32_sdwa v56, 3, v60 dst_sel:DWORD dst_unused:UNUSED_PAD src0_sel:DWORD src1_sel:WORD_0\n\t" \
  "v_lshlrev_b32_sdwa v57, 3, v60 dst_sel:DWORD dst_unused:UNUSED_PAD src0_sel:DWORD src1_sel:WORD_1\n\t" \
  "v_lshlrev_b32_sdwa v58, 3, v61 dst_sel:DWORD dst_unused:UNUSED_PAD src0_sel:DWORD src1_sel:WORD_0\n\t" \
  "v_lshlrev_b32_sdwa v59, 3, v61 dst_sel:DWORD dst_unused:UNUSED_PAD src0_sel:DWORD src1_sel:WORD_1\n\t" \
  "v_lshlrev_b32_sdwa v60, 3, v62 dst_sel:DWORD dst_unused:UNUSED_PAD src0_sel:DWORD src1_sel:WORD_0\n\t" \
  "v_lshlrev_b32_sdwa v61, 3, v62 dst_sel:DWORD dst_unused:UNUSED_PAD src0_sel:DWORD src1_sel:WORD_1\n\t" \
  "v_lshlrev_b32_sdwa v62, 3, v63 dst_sel:DWORD dst_unused:UNUSED_PAD src0_sel:DWORD src1_sel:WORD_0\n\t" \
  "v_lshlrev_b32_sdwa v63, 3, v63 dst_sel:DWORD dst_unused:UNUSED_PAD src0_sel:DWORD src1_sel:WORD_1\n\t" \
  "s_waitcnt vmcnt(5)\n\t" \
  "v_max3_u32 v24, v24, v68, v69\n\t" \
  "v_max3_u32 v24, v24, v70, v71\n\t" \
  "v_lshlrev_b32_sdwa v64, 3, v68 dst_sel:DWORD dst_unused:UNUSED_PAD src0_sel:DWORD src1_sel:WORD_0\n\t" \
  "v_lshlrev_b32_sdwa v65, 3, v68 dst_sel:DWORD dst_unused:UNUSED_PAD src0_sel:DWORD src1_sel:WORD_1\n\t" \
  "v_lshlrev_b32_sdwa v66, 3, v69 dst_sel:DWORD dst_unused:UNUSED_PAD src0_sel:DWORD src1_sel:WORD_0\n\t" \
  "v_lshlrev_b32_sdwa v67, 3, v69 dst_sel:DWORD dst_unused:UNUSED_PAD src0_sel:DWORD src1_sel:WORD_1\n\t" \
  "v_lshlrev_b32_sdwa v68, 3, v70 dst_sel:DWORD dst_unused:UNUSED_PAD src0_sel:DWORD src1_sel:WORD_0\n\t" \
  "v_lshlrev_b32_sdwa v69, 3, v70 dst_sel:DWORD dst_unused:UNUSED_PAD src0_sel:DWORD src1_sel:WORD_1\n\t" \
  "v_lshlrev_b32_sdwa v70, 3, v71 dst_sel:DWORD dst_unused:UNUSED_PAD src0_sel:DWORD src1_sel:WORD_0\n\t" \
  "v_lshlrev_b32_sdwa v71, 3, v71 dst_sel:DWORD dst_unused:UNUSED_PAD src0_sel:DWORD src1_sel:WORD_1\n\t" \
  "s_waitcnt vmcnt(4)\n\t" \
  "v_max3_u32 v24, v24, v76, v77\n\t" \
  "v_max3_u32 v24, v24, v78, v79\n\t" \
  "v_lshlrev_b32_sdwa v72, 3, v76 dst_sel:DWORD dst_unused:UNUSED_PAD src0_sel:DWORD src1_sel:WORD_0\n\t" \
  "v_lshlrev_b32_sdwa v73, 3, v76 dst_sel:DWORD dst_unused:UNUSED_PAD src0_sel:DWORD src1_sel:WORD_1\n\t" \
  "v_lshlrev_b32_sdwa v74, 3, v77 dst_sel:DWORD dst_unused:UNUSED_PAD src0_sel:DWORD src1_sel:WORD_0\n\t" \
  "v_lshlrev_b32_sdwa v75, 3, v77 dst_sel:DWORD dst_unused:UNUSED_PAD src0_sel:DWORD src1_sel:WORD_1\n\t" \
  "v_lshlrev_b32_sdwa v76, 3, v78 dst_sel:DWORD dst_unused:UNUSED_PAD src0_sel:DWORD src1_sel:WORD_0\n\t" \
  "v_lshlrev_b32_sdwa v77, 3, v78 dst_sel:DWORD dst_unused:UNUSED_PAD src0_sel:DWORD src1_sel:WORD_1\n\t" \
  "v_lshlrev_b32_sdwa v78, 3, v79 dst_sel:DWORD dst_unused:UNUSED_PAD src0_sel:DWORD src1_sel:WORD_0\n\t" \
  "v_lshlrev_b32_sdwa v79, 3, v79 dst_sel:DWORD dst_unused:UNUSED_PAD src0_sel:DWORD src1_sel:WORD_1\n\t" \
  "s_waitcnt vmcnt(3)\n\t" \
  "v_max3_u32 v24, v24, v84, v85\n\t" \
  "v_max3_u32 v24, v24, v86, v87\n\t" \
  "v_lshlrev_b32_sdwa v80, 3, v84 dst_sel:DWORD dst_unused:UNUSED_PAD src0_sel:DWORD src1_sel:WORD_0\n\t" \
  "v_lshlrev_b32_sdwa v81, 3, v84 dst_sel:DWORD dst_unused:UNUSED_PAD src0_sel:DWORD src1_sel:WORD_1\n\t" \
  "v_lshlrev_b32_sdwa v82, 3, v85 dst_sel:DWORD dst_unused:UNUSED_PAD src0_sel:DWORD src1_sel:WORD_0\n\t" \
  "v_lshlrev_b32_sdwa v83, 3, v85 dst_sel:DWORD dst_unused:UNUSED_PAD src0_sel:DWORD src1_sel:WORD_1\n\t" \
  "v_lshlrev_b32_sdwa v84, 3, v86 dst_sel:DWORD dst_unused:UNUSED_PAD src0_sel:DWORD src1_sel:WORD_0\n\t" \
  "v_lshlrev_b32_sdwa v85, 3, v86 dst_sel:DWORD dst_unused:UNUSED_PAD src0_sel:DWORD src1_sel:WORD_1\n\t" \
  "v_lshlrev_b32_sdwa v86, 3, v87 dst_sel:DWORD dst_unused:UNUSED_PAD src0_sel:DWORD src1_sel:WORD_0\n\t" \
  "v_lshlrev_b32_sdwa v87, 3, v87 dst_sel:DWORD dst_unused:UNUSED_PAD src0_sel:DWORD src1_sel:WORD_1\n\t" \
  "s_waitcnt vmcnt(2)\n\t" \
  "v_max3_u32 v24, v24, v92, v93\n\t" \
  "v_max3_u32 v24, v24, v94, v95\n\t" \
  "v_lshlrev_b32_sdwa v88, 3, v92 dst_sel:DWORD dst_unused:UNUSED_PAD src0_sel:DWORD src1_sel:WORD_0\n\t" \
  "v_lshlrev_b32_sdwa v89, 3, v92 dst_sel:DWORD dst_unused:UNUSED_PAD src0_sel:DWORD src1_sel:WORD_1\n\t" \
  "v_lshlrev_b32_sdwa v90, 3, v93 dst_sel:DWORD dst_unused:UNUSED_PAD src0_sel:DWORD src1_sel:WORD_0\n\t" \
  "v_lshlrev_b32_sdwa v91, 3, v93 dst_sel:DWORD dst_unused:UNUSED_PAD src0_sel:DWORD src1_sel:WORD_1\n\t" \
  "v_lshlrev_b32_sdwa v92, 3, v94 dst_sel:DWORD dst_unused:UNUSED_PAD src0_sel:DWORD src1_sel:WORD_0\n\t" \
  "v_lshlrev_b32_sdwa v93, 3, v94 dst_sel:DWORD dst_unused:UNUSED_PAD src0_sel:DWORD src1_sel:WORD_1\n\t" \
  "v_lshlrev_b32_sdwa v94, 3, v95 dst_sel:DWORD dst_unused:UNUSED_PAD src0_sel:DWORD src1_sel:WORD_0\n\t" \
  "v_lshlrev_b32_sdwa v95, 3, v95 dst_sel:DWORD dst_unused:UNUSED_PAD src0_sel:DWORD src1_sel:WORD_1\n\t" \
  "s_waitcnt vmcnt(1)\n\t" \
  "v_max3_u32 v24, v24, v100, v101\n\t" \
  "v_max3_u32 v24, v24, v102, v103\n\t" \
  "v_lshlrev_b32_sdwa v96, 3, v100 dst_sel:DWORD dst_unused:UNUSED_PAD src0_sel:DWORD src1_sel:WORD_0\n\t" \
  "v_lshlrev_b32_sdwa v97, 3, v100 dst_sel:DWORD dst_unused:UNUSED_PAD src0_sel:DWORD src1_sel:WORD_1\n\t" \
  "v_lshlrev_b32_sdwa v98, 3, v101 dst_sel:DWORD dst_unused:UNUSED_PAD src0_sel:DWORD src1_sel:WORD_0\n\t" \
  "v_lshlrev_b32_sdwa v99, 3, v101 dst_sel:DWORD dst_unused:UNUSED_PAD src0_sel:DWORD src1_sel:WORD_1\n\t" \
  "v_lshlrev_b32_sdwa v100, 3, v102 dst_sel:DWORD dst_unused:UNUSED_PAD src0_sel:DWORD src1_sel:WORD_0\n\t" \
  "v_lshlrev_b32_sdwa v101, 3, v102 dst_sel:DWORD dst_unused:UNUSED_PAD src0_sel:DWORD src1_sel:WORD_1\n\t" \
  "v_lshlrev_b32_sdwa v102, 3, v103 dst_sel:DWORD dst_unused:UNUSED_PAD src0_sel:DWORD src1_sel:WORD_0\n\t" \
  "v_lshlrev_b32_sdwa v103, 3, v103 dst_sel:DWORD dst_unused:UNUSED_PAD src0_sel:DWORD src1_sel:WORD_1\n\t" \
  "s_waitcnt vmcnt(0)\n\t" \
  "v_max3_u32 v24, v24, v108, v109\n\t" \
  "v_max3_u32 v24, v24, v110, v111\n\t" \
  "v_lshlrev_b32_sdwa v104, 3, v108 dst_sel:DWORD dst_unused:UNUSED_PAD src0_sel:DWORD src1_sel:WORD_0\n\t" \
  "v_lshlrev_b32_sdwa v105, 3, v108 dst_sel:DWORD dst_unused:UNUSED_PAD src0_sel:DWORD src1_sel:WORD_1\n\t" \
  "v_lshlrev_b32_sdwa v106, 3, v109 dst_sel:DWORD dst_unused:UNUSED_PAD src0_sel:DWORD src1_sel:WORD_0\n\t" \
  "v_lshlrev_b32_sdwa v107, 3, v109 dst_sel:DWORD dst_unused:UNUSED_PAD src0_sel:DWORD src1_sel:WORD_1\n\t" \
  "v_lshlrev_b32_sdwa v108, 3, v110 dst_sel:DWORD dst_unused:UNUSED_PAD src0_sel:DWORD src1_sel:WORD_0\n\t" \
  "v_lshlrev_b32_sdwa v109, 3, v110 dst_sel:DWORD dst_unused:UNUSED_PAD src0_sel:DWORD src1_sel:WORD_1\n\t" \
  "v_lshlrev_b32_sdwa v110, 3, v111 dst_sel:DWORD dst_unused:UNUSED_PAD src0_sel:DWORD src1_sel:WORD_0\n\t" \
  "v_lshlrev_b32_sdwa v111, 3, v111 dst_sel:DWORD dst_unused:UNUSED_PAD src0_sel:DWORD src1_sel:WORD_1\n\t" \
  "v_cmp_eq_u32 vcc, -1, v24\n\t" \
  "s_cbranch_vccz .Lgo_%=\n\t" \
  "s_add_u32 %3, %3, 1\n\t" \
  "s_cmp_gt_u32 %3, 0x80000\n\t" \
  "s_cbranch_scc0 .Lsc_%=\n\t" \
  ".Lgo_%=:\n\t" \
  "ds_read_b64 v[24:25], v22 offset:32\n\t" \
  "s_waitcnt lgkmcnt(0)\n\t" \
  "v_cmp_eq_u32 vcc, 16, v25\n\t" \
  "s_cbranch_vccnz .Lg2_%=\n\t" \
  "s_sleep 1\n\t" \
  "s_branch .Lgo_%=\n\t" \
  ".Lg2_%=:\n\t" \
  "v_cmp_ne_u32 vcc, 0, v24\n\t" \
  "s_cbranch_vccnz .Lslow_%=\n\t" \
  "s_cmp_eq_u32 %11, 0\n\t" \
  "s_cbranch_scc1 .Lfirst_%=\n\t" \
  "s_barrier\n\t" \
  "s_cmp_lg_u32 %26, 196608\n\t" \
  "s_cbranch_scc1 .Lg3_%=\n\t" \
  "s_or_b32 %2, %22, 0x100\n\t" \
  "v_mov_b32 v24, %2\n\t" \
  "s_mov_b64 exec, 1\n\t" \
  "buffer_store_dword v24, off, %10, %20 sc1\n\t" \
  "s_mov_b64 exec, -1\n\t" \
  ".Lg3_%=:\n\t" \
  "s_branch .Lfirst_%=\n\t" \
  ".Lloop_%=:\n\t" \
  "s_mov_b32 %6, %11\n\t" \
  "s_lshl_b32 %7, %1, 2\n\t" \
  "s_lshr_b32 %7, %12, %7\n\t" \
  "s_and_b32 %7, %7, 15\n\t" \
  "s_waitcnt lgkmcnt(0)\n\t" \
  "s_barrier\n\t" \
  ".Lfirst_%=:\n\t" \
  "s_add_u32 %0, %0, %13\n\t" \
  "s_add_u32 %2, %1, 24\n\t" \
  "s_bitcmp1_b32 %12, %2\n\t" \
  "s_cbranch_scc1 .Lxt_%=\n\t" \
  ".Lxr_%=:\n\t" \
  "ds_read_b64 v[112:113], v32 offset:32768\n\t" \
  "ds_read_b64 v[114:115], v33 offset:32768\n\t" \
  "ds_read_b64 v[116:117], v34 offset:32768\n\t" \
  "ds_read_b64 v[118:119], v35 offset:32768\n\t" \
  "ds_read_b64 v[120:121], v36 offset:32768\n\t" \
  "ds_read_b64 v[122:123], v37 offset:32768\n\t" \
  "ds_read_b64 v[124:125], v38 offset:32768\n\t" \
  "ds_read_b64 v[126:127], v39 offset:32768\n\t" \
  "s_waitcnt lgkmcnt(7)\n\t" \
  "v_mfma_f32_4x4x4_16b_bf16 v[8:11], v[0:1], v[112:113], 0\n\t" \
  "ds_read_b64 v[112:113], v40 offset:32768\n\t" \
  "s_waitcnt lgkmcnt(7)\n\t" \
  "v_mfma_f32_4x4x4_16b_bf16 v[8:11], v[0:1], v[114:115], v[8:11]\n\t" \
  "ds_read_b64 v[114:115], v41 offset:32768\n\t" \
  "s_waitcnt lgkmcnt(7)\n\t" \
  "v_mfma_f32_4x4x4_16b_bf16 v[8:11], v[0:1], v[116:117], v[8:11]\n\t" \
  "ds_read_b64 v[116:117], v42 offset:32768\n\t" \
  "s_waitcnt lgkmcnt(7)\n\t" \
  "v_mfma_f32_4x4x4_16b_bf16 v[8:11], v[0:1], v[118:119], v[8:11]\n\t" \
  "ds_read_b64 v[118:119], v43 offset:32768\n\t" \
  "s_waitcnt lgkmcnt(7)\n\t" \
  "v_mfma_f32_4x4x4_16b_bf16 v[8:11], v[0:1], v[120:121], v[8:11]\n\t" \
  "ds_read_b64 v[120:121], v44 offset:32768\n\t" \
  "s_waitcnt lgkmcnt(7)\n\t" \
  "v_mfma_f32_4x4x4_16b_bf16 v[8:11], v[0:1], v[122:123], v[8:11]\n\t" \
  "ds_read_b64 v[122:123], v45 offset:32768\n\t" \
  "s_waitcnt lgkmcnt(7)\n\t" \
  "v_mfma_f32_4x4x4_16b_bf16 v[8:11], v[0:1], v[124:125], v[8:11]\n\t" \
  "ds_read_b64 v[124:125], v46 offset:32768\n\t" \
  "s_waitcnt lgkmcnt(7)\n\t" \
  "v_mfma_f32_4x4x4_16b_bf16 v[8:11], v[0:1], v[126:127], v[8:11]\n\t" \
  "ds_read_b64 v[126:127], v47 offset:32768\n\t" \
  "s_waitcnt lgkmcnt(7)\n\t" \
  "v_mfma_f32_4x4x4_16b_bf16 v[8:11], v[0:1], v[112:113], v[8:11]\n\t" \
  "ds_read_b64 v[112:113], v48 offset:32768\n\t" \
  "s_waitcnt lgkmcnt(7)\n\t" \
  "v_mfma_f32_4x4x4_16b_bf16 v[8:11], v[0:1], v[114:115], v[8:11]\n\t" \
  "ds_read_b64 v[114:115], v49 offset:32768\n\t" \
  "s_waitcnt lgkmcnt(7)\n\t" \
  "v_mfma_f32_4x4x4_16b_bf16 v[8:11], v[0:1], v[116:117], v[8:11]\n\t" \
  "ds_read_b64 v[116:117], v50 offset:32768\n\t" \
  "s_waitcnt lgkmcnt(7)\n\t" \
  "v_mfma_f32_4x4x4_16b_bf16 v[8:11], v[0:1], v[118:119], v[8:11]\n\t" \
  "ds_read_b64 v[118:119], v51 offset:32768\n\t" \
  "s_waitcnt lgkmcnt(7)\n\t" \
  "v_mfma_f32_4x4x4_16b_bf16 v[8:11], v[0:1], v[120:121], v[8:11]\n\t" \
  "ds_read_b64 v[120:121], v52 offset:32768\n\t" \
  "s_waitcnt lgkmcnt(7)\n\t" \
  "v_mfma_f32_4x4x4_16b_bf16 v[8:11], v[0:1], v[122:123], v[8:11]\n\t" \
  "ds_read_b64 v[122:123], v53 offset:32768\n\t" \
  "s_waitcnt lgkmcnt(7)\n\t" \
  "v_mfma_f32_4x4x4_16b_bf16 v[8:11], v[0:1], v[124:125], v[8:11]\n\t" \
  "ds_read_b64 v[124:125], v54 offset:32768\n\t" \
  "s_waitcnt lgkmcnt(7)\n\t" \
  "v_mfma_f32_4x4x4_16b_bf16 v[8:11], v[0:1], v[126:127], v[8:11]\n\t" \
  "ds_read_b64 v[126:127], v55 offset:32768\n\t" \
  "s_waitcnt lgkmcnt(7)\n\t" \
  "v_mfma_f32_4x4x4_16b_bf16 v[8:11], v[0:1], v[112:113], v[8:11]\n\t" \
  "ds_read_b64 v[112:113], v56 offset:32768\n\t" \
  "s_waitcnt lgkmcnt(7)\n\t" \
  "v_mfma_f32_4x4x4_16b_bf16 v[8:11], v[0:1], v[114:115], v[8:11]\n\t" \
  "ds_read_b64 v[114:115], v57 offset:32768\n\t" \
  "s_waitcnt lgkmcnt(7)\n\t" \
  "v_mfma_f32_4x4x4_16b_bf16 v[8:11], v[0:1], v[116:117], v[8:11]\n\t" \
  "ds_read_b64 v[116:117], v58 offset:32768\n\t" \
  "s_waitcnt lgkmcnt(7)\n\t" \
  "v_mfma_f32_4x4x4_16b_bf16 v[8:11], v[0:1], v[118:119], v[8:11]\n\t" \
  "ds_read_b64 v[118:119], v59 offset:32768\n\t" \
  "s_waitcnt lgkmcnt(7)\n\t" \
  "v_mfma_f32_4x4x4_16b_bf16 v[8:11], v[0:1], v[120:121], v[8:11]\n\t" \
  "ds_read_b64 v[120:121], v60 offset:32768\n\t" \
  "s_waitcnt lgkmcnt(7)\n\t" \
  "v_mfma_f32_4x4x4_16b_bf16 v[8:11], v[0:1], v[122:123], v[8:11]\n\t" \
  "ds_read_b64 v[122:123], v61 offset:32768\n\t" \
  "s_waitcnt lgkmcnt(7)\n\t" \
  "v_mfma_f32_4x4x4_16b_bf16 v[8:11], v[0:1], v[124:125], v[8:11]\n\t" \
  "ds_read_b64 v[124:125], v62 offset:32768\n\t" \
  "s_waitcnt lgkmcnt(7)\n\t" \
  "v_mfma_f32_4x4x4_16b_bf16 v[8:11], v[0:1], v[126:127], v[8:11]\n\t" \
  "ds_read_b64 v[126:127], v63 offset:32768\n\t" \
  "s_waitcnt lgkmcnt(7)\n\t" \
  "v_mfma_f32_4x4x4_16b_bf16 v[8:11], v[0:1], v[112:113], v[8:11]\n\t" \
  "ds_read_b64 v[112:113], v64 offset:32768\n\t" \
  "s_waitcnt lgkmcnt(7)\n\t" \
  "v_mfma_f32_4x4x4_16b_bf16 v[8:11], v[0:1], v[114:115], v[8:11]\n\t" \
  "ds_read_b64 v[114:115], v65 offset:32768\n\t" \
  "s_waitcnt lgkmcnt(7)\n\t" \
  "v_mfma_f32_4x4x4_16b_bf16 v[8:11], v[0:1], v[116:117], v[8:11]\n\t" \
  "ds_read_b64 v[116:117], v66 offset:32768\n\t" \
  "s_waitcnt lgkmcnt(7)\n\t" \
  "v_mfma_f32_4x4x4_16b_bf16 v[8:11], v[0:1], v[118:119], v[8:11]\n\t" \
  "ds_read_b64 v[118:119], v67 offset:32768\n\t" \
  "s_waitcnt lgkmcnt(7)\n\t" \
  "v_mfma_f32_4x4x4_16b_bf16 v[8:11], v[0:1], v[120:121], v[8:11]\n\t" \
  "ds_read_b64 v[120:121], v68 offset:32768\n\t" \
  "s_waitcnt lgkmcnt(7)\n\t" \
  "v_mfma_f32_4x4x4_16b_bf16 v[8:11], v[0:1], v[122:123], v[8:11]\n\t" \
  "ds_read_b64 v[122:123], v69 offset:32768\n\t" \
  "s_waitcnt lgkmcnt(7)\n\t" \
  "v_mfma_f32_4x4x4_16b_bf16 v[8:11], v[0:1], v[124:125], v[8:11]\n\t" \
  "ds_read_b64 v[124:125], v70 offset:32768\n\t" \
  "s_waitcnt lgkmcnt(7)\n\t" \
  "v_mfma_f32_4x4x4_16b_bf16 v[8:11], v[0:1], v[126:127], v[8:11]\n\t" \
  "ds_read_b64 v[126:127], v71 offset:32768\n\t" \
  "s_waitcnt lgkmcnt(7)\n\t" \
  "v_mfma_f32_4x4x4_16b_bf16 v[8:11], v[0:1], v[112:113], v[8:11]\n\t" \
  "ds_read_b64 v[112:113], v72 offset:32768\n\t" \
  "s_waitcnt lgkmcnt(7)\n\t" \
  "v_mfma_f32_4x4x4_16b_bf16 v[8:11], v[0:1], v[114:115], v[8:11]\n\t" \
  "ds_read_b64 v[114:115], v73 offset:32768\n\t" \
  "s_waitcnt lgkmcnt(7)\n\t" \
  "v_mfma_f32_4x4x4_16b_bf16 v[8:11], v[0:1], v[116:117], v[8:11]\n\t" \
  "ds_read_b64 v[116:117], v74 offset:32768\n\t" \
  "s_waitcnt lgkmcnt(7)\n\t" \
  "v_mfma_f32_4x4x4_16b_bf16 v[8:11], v[0:1], v[118:119], v[8:11]\n\t" \
  "ds_read_b64 v[118:119], v75 offset:32768\n\t" \
  "s_waitcnt lgkmcnt(7)\n\t" \
  "v_mfma_f32_4x4x4_16b_bf16 v[8:11], v[0:1], v[120:121], v[8:11]\n\t" \
  "ds_read_b64 v[120:121], v76 offset:32768\n\t" \
  "s_waitcnt lgkmcnt(7)\n\t" \
  "v_mfma_f32_4x4x4_16b_bf16 v[8:11], v[0:1], v[122:123], v[8:11]\n\t" \
  "ds_read_b64 v[122:123], v77 offset:32768\n\t" \
  "s_waitcnt lgkmcnt(7)\n\t" \
  "v_mfma_f32_4x4x4_16b_bf16 v[8:11], v[0:1], v[124:125], v[8:11]\n\t" \
  "ds_read_b64 v[124:125], v78 offset:32768\n\t" \
  "s_waitcnt lgkmcnt(7)\n\t" \
  "v_mfma_f32_4x4x4_16b_bf16 v[8:11], v[0:1], v[126:127], v[8:11]\n\t" \
  "ds_read_b64 v[126:127], v79 offset:32768\n\t" \
  "s_waitcnt lgkmcnt(7)\n\t" \
  "v_mfma_f32_4x4x4_16b_bf16 v[12:15], v[0:1], v[112:113], 0\n\t" \
  "ds_read_b64 v[112:113], v80 offset:32768\n\t" \
  "s_waitcnt lgkmcnt(7)\n\t" \
  "v_mfma_f32_4x4x4_16b_bf16 v[12:15], v[0:1], v[114:115], v[12:15]\n\t" \
  "ds_read_b64 v[114:115], v81 offset:32768\n\t" \
  "s_waitcnt lgkmcnt(7)\n\t" \
  "v_mfma_f32_4x4x4_16b_bf16 v[12:15], v[0:1], v[116:117], v[12:15]\n\t" \
  "ds_read_b64 v[116:117], v82 offset:32768\n\t" \
  "s_waitcnt lgkmcnt(7)\n\t" \
  "v_mfma_f32_4x4x4_16b_bf16 v[12:15], v[0:1], v[118:119], v[12:15]\n\t" \
  "ds_read_b64 v[118:119], v83 offset:32768\n\t" \
  "s_waitcnt lgkmcnt(7)\n\t" \
  "v_mfma_f32_4x4x4_16b_bf16 v[12:15], v[0:1], v[120:121], v[12:15]\n\t" \
  "ds_read_b64 v[120:121], v84 offset:32768\n\t" \
  "s_waitcnt lgkmcnt(7)\n\t" \
  "v_mfma_f32_4x4x4_16b_bf16 v[12:15], v[0:1], v[122:123], v[12:15]\n\t" \
  "ds_read_b64 v[122:123], v85 offset:32768\n\t" \
  "s_waitcnt lgkmcnt(7)\n\t" \
  "v_mfma_f32_4x4x4_16b_bf16 v[12:15], v[0:1], v[124:125], v[12:15]\n\t" \
  "ds_read_b64 v[124:125], v86 offset:32768\n\t" \
  "s_waitcnt lgkmcnt(7)\n\t" \
  "v_mfma_f32_4x4x4_16b_bf16 v[12:15], v[0:1], v[126:127], v[12:15]\n\t" \
  "ds_read_b64 v[126:127], v87 offset:32768\n\t" \
  "s_waitcnt lgkmcnt(7)\n\t" \
  "v_mfma_f32_4x4x4_16b_bf16 v[12:15], v[0:1], v[112:113], v[12:15]\n\t" \
  "ds_read_b64 v[112:113], v88 offset:32768\n\t" \
  "s_waitcnt lgkmcnt(7)\n\t" \
  "v_mfma_f32_4x4x4_16b_bf16 v[12:15], v[0:1], v[114:115], v[12:15]\n\t" \
  "ds_read_b64 v[114:115], v89 offset:32768\n\t" \
  "s_waitcnt lgkmcnt(7)\n\t" \
  "v_mfma_f32_4x4x4_16b_bf16 v[12:15], v[0:1], v[116:117], v[12:15]\n\t" \
  "ds_read_b64 v[116:117], v90 offset:32768\n\t" \
  "s_waitcnt lgkmcnt(7)\n\t" \
  "v_mfma_f32_4x4x4_16b_bf16 v[12:15], v[0:1], v[118:119], v[12:15]\n\t" \
  "ds_read_b64 v[118:119], v91 offset:32768\n\t" \
  "s_waitcnt lgkmcnt(7)\n\t" \
  "v_mfma_f32_4x4x4_16b_bf16 v[12:15], v[0:1], v[120:121], v[12:15]\n\t" \
  "ds_read_b64 v[120:121], v92 offset:32768\n\t" \
  "s_waitcnt lgkmcnt(7)\n\t" \
  "v_mfma_f32_4x4x4_16b_bf16 v[12:15], v[0:1], v[122:123], v[12:15]\n\t" \
  "ds_read_b64 v[122:123], v93 offset:32768\n\t" \
  "s_waitcnt lgkmcnt(7)\n\t" \
  "v_mfma_f32_4x4x4_16b_bf16 v[12:15], v[0:1], v[124:125], v[12:15]\n\t" \
  "ds_read_b64 v[124:125], v94 offset:32768\n\t" \
  "s_waitcnt lgkmcnt(7)\n\t" \
  "v_mfma_f32_4x4x4_16b_bf16 v[12:15], v[0:1], v[126:127], v[12:15]\n\t" \
  "ds_read_b64 v[126:127], v95 offset:32768\n\t" \
  "s_waitcnt lgkmcnt(7)\n\t" \
  "v_mfma_f32_4x4x4_16b_bf16 v[12:15], v[0:1], v[112:113], v[12:15]\n\t" \
  "ds_read_b64 v[112:113], v96 offset:32768\n\t" \
  "s_waitcnt lgkmcnt(7)\n\t" \
  "v_mfma_f32_4x4x4_16b_bf16 v[12:15], v[0:1], v[114:115], v[12:15]\n\t" \
  "ds_read_b64 v[114:115], v97 offset:32768\n\t" \
  "s_waitcnt lgkmcnt(7)\n\t" \
  "v_mfma_f32_4x4x4_16b_bf16 v[12:15], v[0:1], v[116:117], v[12:15]\n\t" \
  "ds_read_b64 v[116:117], v98 offset:32768\n\t" \
  "s_waitcnt lgkmcnt(7)\n\t" \
  "v_mfma_f32_4x4x4_16b_bf16 v[12:15], v[0:1], v[118:119], v[12:15]\n\t" \
  "ds_read_b64 v[118:119], v99 offset:32768\n\t" \
  "s_waitcnt lgkmcnt(7)\n\t" \
  "v_mfma_f32_4x4x4_16b_bf16 v[12:15], v[0:1], v[120:121], v[12:15]\n\t" \
  "ds_read_b64 v[120:121], v100 offset:32768\n\t" \
  "s_waitcnt lgkmcnt(7)\n\t" \
  "v_mfma_f32_4x4x4_16b_bf16 v[12:15], v[0:1], v[122:123], v[12:15]\n\t" \
  "ds_read_b64 v[122:123], v101 offset:32768\n\t" \
  "s_waitcnt lgkmcnt(7)\n\t" \
  "v_mfma_f32_4x4x4_16b_bf16 v[12:15], v[0:1], v[124:125], v[12:15]\n\t" \
  "ds_read_b64 v[124:125], v102 offset:32768\n\t" \
  "s_waitcnt lgkmcnt(7)\n\t" \
  "v_mfma_f32_4x4x4_16b_bf16 v[12:15], v[0:1], v[126:127], v[12:15]\n\t" \
  "ds_read_b64 v[126:127], v103 offset:32768\n\t" \
  "s_waitcnt lgkmcnt(7)\n\t" \
  "v_mfma_f32_4x4x4_16b_bf16 v[12:15], v[0:1], v[112:113], v[12:15]\n\t" \
  "ds_read_b64 v[112:113], v104 offset:32768\n\t" \
  "s_waitcnt lgkmcnt(7)\n\t" \
  "v_mfma_f32_4x4x4_16b_bf16 v[12:15], v[0:1], v[114:115], v[12:15]\n\t" \
  "ds_read_b64 v[114:115], v105 offset:32768\n\t" \
  "s_waitcnt lgkmcnt(7)\n\t" \
  "v_mfma_f32_4x4x4_16b_bf16 v[12:15], v[0:1], v[116:117], v[12:15]\n\t" \
  "ds_read_b64 v[116:117], v106 offset:32768\n\t" \
  "s_waitcnt lgkmcnt(7)\n\t" \
  "v_mfma_f32_4x4x4_16b_bf16 v[12:15], v[0:1], v[118:119], v[12:15]\n\t" \
  "ds_read_b64 v[118:119], v107 offset:32768\n\t" \
  "s_waitcnt lgkmcnt(7)\n\t" \
  "v_mfma_f32_4x4x4_16b_bf16 v[12:15], v[0:1], v[120:121], v[12:15]\n\t" \
  "ds_read_b64 v[120:121], v108 offset:32768\n\t" \
  "s_waitcnt lgkmcnt(7)\n\t" \
  "v_mfma_f32_4x4x4_16b_bf16 v[12:15], v[0:1], v[122:123], v[12:15]\n\t" \
  "ds_read_b64 v[122:123], v109 offset:32768\n\t" \
  "s_waitcnt lgkmcnt(7)\n\t" \
  "v_mfma_f32_4x4x4_16b_bf16 v[12:15], v[0:1], v[124:125], v[12:15]\n\t" \
  "ds_read_b64 v[124:125], v110 offset:32768\n\t" \
  "s_waitcnt lgkmcnt(7)\n\t" \
  "v_mfma_f32_4x4x4_16b_bf16 v[12:15], v[0:1], v[126:127], v[12:15]\n\t" \
  "ds_read_b64 v[126:127], v111 offset:32768\n\t" \
  "s_waitcnt lgkmcnt(7)\n\t" \
  "v_mfma_f32_4x4x4_16b_bf16 v[12:15], v[0:1], v[112:113], v[12:15]\n\t" \
  "s_mov_b64 exec, %4\n\t" \
  "v_add_f32_dpp v8, v8, v8 quad_perm:[1,0,3,2] row_mask:0xf bank_mask:0xf\n\t" \
  "v_add_f32_dpp v9, v9, v9 quad_perm:[1,0,3,2] row_mask:0xf bank_mask:0xf\n\t" \
  "v_add_f32_dpp v10, v10, v10 quad_perm:[1,0,3,2] row_mask:0xf bank_mask:0xf\n\t" \
  "v_add_f32_dpp v11, v11, v11 quad_perm:[1,0,3,2] row_mask:0xf bank_mask:0xf\n\t" \
  "s_mov_b64 exec, -1\n\t" \
  "s_waitcnt lgkmcnt(6)\n\t" \
  "v_mfma_f32_4x4x4_16b_bf16 v[12:15], v[0:1], v[114:115], v[12:15]\n\t" \
  "v_mul_f32 v8, v3, v8\n\t" \
  "v_mul_f32 v9, v3, v9\n\t" \
  "s_waitcnt lgkmcnt(5)\n\t" \
  "v_mfma_f32_4x4x4_16b_bf16 v[12:15], v[0:1], v[116:117], v[12:15]\n\t" \
  "v_mul_f32 v10, v3, v10\n\t" \
  "v_mul_f32 v11, v3, v11\n\t" \
  "s_waitcnt lgkmcnt(4)\n\t" \
  "v_mfma_f32_4x4x4_16b_bf16 v[12:15], v[0:1], v[118:119], v[12:15]\n\t" \
  "v_cvt_pk_bf16_f32 v8, v8, v9\n\t" \
  "s_waitcnt lgkmcnt(3)\n\t" \
  "v_mfma_f32_4x4x4_16b_bf16 v[12:15], v[0:1], v[120:121], v[12:15]\n\t" \
  "v_cvt_pk_bf16_f32 v9, v10, v11\n\t" \
  "s_waitcnt lgkmcnt(2)\n\t" \
  "v_mfma_f32_4x4x4_16b_bf16 v[12:15], v[0:1], v[122:123], v[12:15]\n\t" \
  "v_min_u32 v8, -2, v8\n\t" \
  "s_waitcnt lgkmcnt(1)\n\t" \
  "v_mfma_f32_4x4x4_16b_bf16 v[12:15], v[0:1], v[124:125], v[12:15]\n\t" \
  "v_min_u32 v9, -2, v9\n\t" \
  "s_waitcnt lgkmcnt(0)\n\t" \
  "v_mfma_f32_4x4x4_16b_bf16 v[12:15], v[0:1], v[126:127], v[12:15]\n\t" \
  "ds_write_b64 v2, v[8:9]\n\t" \
  "s_mov_b64 exec, %5\n\t" \
  "s_nop 2\n\t" \
  "v_add_f32_dpp v12, v12, v12 quad_perm:[1,0,3,2] row_mask:0xf bank_mask:0xf\n\t" \
  "v_add_f32_dpp v13, v13, v13 quad_perm:[1,0,3,2] row_mask:0xf bank_mask:0xf\n\t" \
  "v_add_f32_dpp v14, v14, v14 quad_perm:[1,0,3,2] row_mask:0xf bank_mask:0xf\n\t" \
  "v_add_f32_dpp v15, v15, v15 quad_perm:[1,0,3,2] row_mask:0xf bank_mask:0xf\n\t" \
  "s_mov_b64 exec, -1\n\t" \
  "v_mul_f32 v12, v5, v12\n\t" \
  "v_mul_f32 v13, v5, v13\n\t" \
  "v_mul_f32 v14, v5, v14\n\t" \
  "v_mul_f32 v15, v5, v15\n\t" \
  "v_cvt_pk_bf16_f32 v12, v12, v13\n\t" \
  "v_cvt_pk_bf16_f32 v13, v14, v15\n\t" \
  "v_min_u32 v12, -2, v12\n\t" \
  "v_min_u32 v13, -2, v13\n\t" \
  "ds_write_b64 v4, v[12:13]\n\t" \
  "ds_read_b128 v[22:25], v6\n\t" \
  "s_mov_b64 exec, %14\n\t" \
  "s_waitcnt lgkmcnt(0)\n\t" \
  "buffer_store_dwordx4 v[22:25], v7, %10, %0 offen sc1\n\t" \
  "s_mov_b64 exec, -1\n\t" \
  "s_cmp_eq_u32 %1, 6\n\t" \
  "s_cbranch_scc1 .Lend_%=\n\t" \
  "s_cmp_eq_u32 %1, 0\n\t" \
  "s_cselect_b32 %2, 1, %6\n\t" \
  "s_cmp_eq_u32 %2, 0\n\t" \
  "s_cbranch_scc1 .Lsl_%=\n\t" \
  "s_barrier\n\t" \
  "s_cmp_lg_u32 %6, 0\n\t" \
  "s_cbranch_scc1 .Lsl_%=\n\t" \
  "v_mov_b32 v112, 153520\n\t" \
  "s_cmp_lg_u32 %21, 6\n\t" \
  "s_cbranch_scc1 .Lh1_%=\n\t" \
  "s_bfe_u32 %2, %20, 0x3000a\n\t" \
  "s_mov_b64 exec, 0xff\n\t" \
  "s_bitset0_b32 exec_lo, %2\n\t" \
  "v_mbcnt_lo_u32_b32 v113, -1, 0\n\t" \
  "v_lshlrev_b32 v113, 10, v113\n\t" \
  "s_andn2_b32 %2, %20, 0x1c00\n\t" \
  ".Lh0_%=:\n\t" \
  "buffer_load_dword v114, v113, %10, %2 offen sc1\n\t" \
  "s_waitcnt vmcnt(0)\n\t" \
  "v_cmp_gt_u32 vcc, 0x100, v114\n\t" \
  "s_cbranch_vccz .Lh2_%=\n\t" \
  "s_add_u32 %7, %7, 1\n\t" \
  "s_cmp_gt_u32 %7, 0x80000\n\t" \
  "s_cbranch_scc1 .Lh2_%=\n\t" \
  "s_sleep 4\n\t" \
  "s_branch .Lh0_%=\n\t" \
  ".Lh2_%=:\n\t" \
  "ds_write_b32 v112, v112 offset:40\n\t" \
  "s_mov_b64 exec, -1\n\t" \
  "s_branch .Lld_%=\n\t" \
  ".Lh1_%=:\n\t" \
  "ds_read_b32 v113, v112 offset:40\n\t" \
  "s_waitcnt lgkmcnt(0)\n\t" \
  "v_cmp_ne_u32 vcc, 0, v113\n\t" \
  "s_cbranch_vccnz .Lld_%=\n\t" \
  "s_sleep 2\n\t" \
  "s_branch .Lh1_%=\n\t" \
  ".Lsl_%=:\n\t" \
  "s_cmp_eq_u32 %7, 0\n\t" \
  "s_cbranch_scc1 .Lld_%=\n\t" \
  "s_sleep 1\n\t" \
  "s_sub_u32 %7, %7, 1\n\t" \
  "s_branch .Lsl_%=\n\t" \
  ".Lld_%=:\n\t" \
  "buffer_load_dwordx4 v[112:115], v16, %10, %0 offen sc1\n\t" \
  "buffer_load_dwordx4 v[116:119], v17, %10, %0 offen sc1\n\t" \
  "buffer_load_dwordx4 v[120:123], v18, %10, %0 offen sc1\n\t" \
  "buffer_load_dwordx4 v[124:127], v19, %10, %0 offen sc1\n\t" \
  "buffer_load_dwordx4 v[8:11], v20, %10, %0 offen sc1\n\t" \
  "v_mov_b32 v12, 0\n\t" \
  "v_mov_b32 v13, 0\n\t" \
  "v_mov_b32 v14, 0\n\t" \
  "v_mov_b32 v15, 0\n\t" \
  "s_mov_b64 exec, 0x7fffff\n\t" \
  "buffer_load_dwordx4 v[12:15], v21, %10, %0 offen sc1\n\t" \
  "s_mov_b64 exec, -1\n\t" \
  "s_cmp_eq_u32 %6, 0\n\t" \
  "s_cbranch_scc0 .Llate_%=\n\t" \
  "s_waitcnt vmcnt(5)\n\t" \
  "v_max_u32 v26, v112, v113\n\t" \
  "v_max3_u32 v26, v26, v114, v115\n\t" \
  "s_waitcnt vmcnt(4)\n\t" \
  "v_max3_u32 v26, v26, v116, v117\n\t" \
  "v_max3_u32 v26, v26, v118, v119\n\t" \
  "s_waitcnt vmcnt(3)\n\t" \
  "v_max3_u32 v26, v26, v120, v121\n\t" \
  "v_max3_u32 v26, v26, v122, v123\n\t" \
  "s_waitcnt vmcnt(2)\n\t" \
  "v_max3_u32 v26, v26, v124, v125\n\t" \
  "v_max3_u32 v26, v26, v126, v127\n\t" \
  "s_waitcnt vmcnt(1)\n\t" \
  "v_max3_u32 v26, v26, v8, v9\n\t" \
  "v_max3_u32 v26, v26, v10, v11\n\t" \
  "s_waitcnt vmcnt(0)\n\t" \
  "v_max3_u32 v26, v26, v12, v13\n\t" \
  "v_max3_u32 v26, v26, v14, v15\n\t" \
  "v_cmp_eq_u32 vcc, -1, v26\n\t" \
  "s_cbranch_vccnz .Lsw_%=\n\t" \
  ".Lok_%=:\n\t" \
  "s_cmp_eq_u32 %6, 0\n\t" \
  "s_cbranch_scc0 .Lown_%=\n\t" \
  "s_barrier\n\t" \
  "ds_write_b128 v16, v[112:115] offset:32768\n\t" \
  "ds_write_b128 v17, v[116:119] offset:32768\n\t" \
  "ds_write_b128 v18, v[120:123] offset:32768\n\t" \
  "ds_write_b128 v19, v[124:127] offset:32768\n\t" \
  "ds_write_b128 v20, v[8:11] offset:32768\n\t" \
  "s_mov_b64 exec, 0x7fffff\n\t" \
  "ds_write_b128 v21, v[12:15] offset:32768\n\t" \
  ".Lown_%=:\n\t" \
  "s_mov_b64 exec, %14\n\t" \
  "ds_write_b128 v7, v[22:25] offset:32768\n\t" \
  "s_mov_b64 exec, -1\n\t" \
  "s_add_u32 %1, %1, 1\n\t" \
  "s_branch .Lloop_%=\n\t" \
  ".Llate_%=:\n\t" \
  "s_waitcnt vmcnt(5)\n\t" \
  "ds_write_b128 v16, v[112:115] offset:32768\n\t" \
  "v_max_u32 v26, v112, v113\n\t" \
  "v_max3_u32 v26, v26, v114, v115\n\t" \
  "s_waitcnt vmcnt(4)\n\t" \
  "ds_write_b128 v17, v[116:119] offset:32768\n\t" \
  "v_max3_u32 v26, v26, v116, v117\n\t" \
  "v_max3_u32 v26, v26, v118, v119\n\t" \
  "s_waitcnt vmcnt(3)\n\t" \
  "ds_write_b128 v18, v[120:123] offset:32768\n\t" \
  "v_max3_u32 v26, v26, v120, v121\n\t" \
  "v_max3_u32 v26, v26, v122, v123\n\t" \
  "s_waitcnt vmcnt(2)\n\t" \
  "ds_write_b128 v19, v[124:127] offset:32768\n\t" \
  "v_max3_u32 v26, v26, v124, v125\n\t" \
  "v_max3_u32 v26, v26, v126, v127\n\t" \
  "s_waitcnt vmcnt(1)\n\t" \
  "ds_write_b128 v20, v[8:11] offset:32768\n\t" \
  "v_max3_u32 v26, v26, v8, v9\n\t" \
  "v_max3_u32 v26, v26, v10, v11\n\t" \
  "s_waitcnt vmcnt(0)\n\t" \
  "s_mov_b64 exec, 0x7fffff\n\t" \
  "ds_write_b128 v21, v[12:15] offset:32768\n\t" \
  "s_mov_b64 exec, -1\n\t" \
  "v_max3_u32 v26, v26, v12, v13\n\t" \
  "v_max3_u32 v26, v26, v14, v15\n\t" \
  "v_cmp_eq_u32 vcc, -1, v26\n\t" \
  "s_cbranch_vccz .Lown_%=\n\t" \
  "s_waitcnt lgkmcnt(0)\n\t" \
  ".Lretry_%=:\n\t" \
  "s_add_u32 %3, %3, 1\n\t" \
  "s_cmp_gt_u32 %3, 0x80000\n\t" \
  "s_cbranch_scc1 .Lok_%=\n\t" \
  "s_sleep 4\n\t" \
  "s_branch .Lld_%=\n\t" \
  ".Lsw_%=:\n\t" \
  "s_branch .Lretry_%=\n\t" \
  ".Lxt_%=:\n\t" \
  "s_setprio 3\n\t" \
  "s_lshl_b32 %2, %1, 2\n\t" \
  "s_lshr_b64 vcc, %25, %2\n\t" \
  "s_and_b32 %2, vcc_lo, 15\n\t" \
  "s_cbranch_scc0 .Lna_%=\n\t" \
  "v_mov_b32 v22, -1\n\t" \
  "v_mov_b32 v23, -1\n\t" \
  "v_mov_b32 v24, -1\n\t" \
  "v_mov_b32 v25, -1\n\t" \
  "s_bitcmp1_b32 %2, 0\n\t" \
  "s_cselect_b32 vcc_lo, 6272, 0\n\t" \
  "s_lshr_b32 %2, %2, 1\n\t" \
  "s_mul_i32 %2, %2, %13\n\t" \
  "s_add_u32 %2, %2, %0\n\t" \
  "s_add_u32 %2, %2, vcc_lo\n\t" \
  "buffer_store_dwordx4 v[22:25], v31, %10, %2 offen sc1\n\t" \
  "s_and_b32 %2, vcc_hi, 15\n\t" \
  "s_cbranch_scc0 .Lna_%=\n\t" \
  "s_bitcmp1_b32 %2, 0\n\t" \
  "s_cselect_b32 vcc_lo, 6272, 0\n\t" \
  "s_lshr_b32 %2, %2, 1\n\t" \
  "s_mul_i32 %2, %2, %13\n\t" \
  "s_add_u32 %2, %2, %0\n\t" \
  "s_add_u32 %2, %2, vcc_lo\n\t" \
  "buffer_store_dwordx4 v[22:25], v31, %10, %2 offen sc1\n\t" \
  ".Lna_%=:\n\t" \
  "s_cmp_lt_u32 %1, %23\n\t" \
  "s_cbranch_scc1 .Lnq_%=\n\t" \
  "s_sub_u32 %2, %1, 1\n\t" \
  "s_mul_i32 %2, %2, %24\n\t" \
  "buffer_load_dword v28, v27, %10, %2 offen sc1\n\t" \
  ".Lnq_%=:\n\t" \
  "s_lshl4_add_u32 %2, %11, %1\n\t" \
  "s_cmp_lg_u32 %2, 21\n\t" \
  "s_cbranch_scc1 .Lnm_%=\n\t" \
  "s_sub_u32 %2, %17, 133376\n\t" \
  "s_lshl_b32 %2, %2, 2\n\t" \
  "s_and_b32 m0, %2, 0x7fff\n\t" \
  "s_nop 0\n\t" \
  "buffer_load_dwordx4 v6, %10, %19 offen offset:0 sc1 lds\n\t" \
  "buffer_load_dwordx4 v6, %10, %19 offen offset:1024 sc1 lds\n\t" \
  "buffer_load_dwordx4 v6, %10, %19 offen offset:2048 sc1 lds\n\t" \
  "buffer_load_dwordx4 v6, %10, %19 offen offset:3072 sc1 lds\n\t" \
  "s_cmp_lg_u32 m0, 0\n\t" \
  "s_cbranch_scc1 .Lnm_%=\n\t" \
  "s_mov_b32 m0, 152224\n\t" \
  "s_add_u32 %2, %19, 65536\n\t" \
  "buffer_load_dwordx4 v6, %10, %2 offen sc1 lds\n\t" \
  "s_mov_b64 exec, 0x1ffff\n\t" \
  "buffer_load_dwordx4 v6, %10, %2 offen offset:1024 sc1 lds\n\t" \
  "s_mov_b64 exec, -1\n\t" \
  ".Lnm_%=:\n\t" \
  "s_cmp_lg_u32 %1, %21\n\t" \
  "s_cbranch_scc1 .Lnp_%=\n\t" \
  "s_or_b32 %2, %22, 0x600\n\t" \
  "v_mov_b32 v26, %2\n\t" \
  "s_mov_b64 exec, 1\n\t" \
  "buffer_store_dword v26, off, %10, %20 sc1\n\t" \
  "s_mov_b64 exec, -1\n\t" \
  ".Lnp_%=:\n\t" \
  "s_setprio 0\n\t" \
  "s_branch .Lxr_%=\n\t" \
  ".Lslow_%=:\n\t" \
  "s_mov_b32 %1, 16\n\t" \
  ".Lend_%=:\n\t" \
  : "+s"(SOFF), "+s"(HOP), "=&s"(TMP), "+s"(RETRY), "=&s"(MK0), "=&s"(MK1), "+s"(ROLE), "+s"(DCUR), "+{v[0:7]}"(M8), "+{v26}"(L16) \
  : "s"(RSRC), "s"(GRP), "s"(DLY), "s"(HS), "s"(M49), "s"(SO), "s"(MO), "s"(SB), "s"(FP), "s"(MS), "s"(FO), "s"(FH), "s"(FL), "s"(PH), "s"(PM), "s"(MT), "s"(BT), "{v[16:21]}"(O6), "{v27}"(PO), "{v31}"(MO2) \
  : "memory", "scc", "vcc", "s90", "s91", "v29", "v30", "v8", "v9", "v10", "v11", "v12", "v13", "v14", "v15", "v22", "v23", "v24", "v25", "v28", "v32", "v33", "v34", "v35", "v36", "v37", "v38", "v39", "v40", "v41", "v42", "v43", "v44", "v45", "v46", "v47", "v48", "v49", "v50", "v51", "v52", "v53", "v54", "v55", "v56", "v57", "v58", "v59", "v60", "v61", "v62", "v63", "v64", "v65", "v66", "v67", "v68", "v69", "v70", "v71", "v72", "v73", "v74", "v75", "v76", "v77", "v78", "v79", "v80", "v81", "v82", "v83", "v84", "v85", "v86", "v87", "v88", "v89", "v90", "v91", "v92", "v93", "v94", "v95", "v96", "v97", "v98", "v99", "v100", "v101", "v102", "v103", "v104", "v105", "v106", "v107", "v108", "v109", "v110", "v111", "v112", "v113", "v114", "v115", "v116", "v117", "v118", "v119", "v120", "v121", "v122", "v123", "v124", "v125", "v126", "v127")

#define PRE_FINAL(T1, T2, W, RSRC, FB, PP, T7) asm volatile( \
  "v_mbcnt_lo_u32_b32 v0, -1, 0\n\t" \
  "v_mbcnt_hi_u32_b32 v0, -1, v0\n\t" \
  "s_mov_b32 %1, 0\n\t" \
  "s_cmp_gt_u32 %2, 7\n\t" \
  "s_cbranch_scc1 .Lb_%=\n\t" \
  "s_cmp_lg_u32 %2, 0\n\t" \
  "s_cbranch_scc1 .Lend_%=\n\t" \
  "v_bfe_u32 v1, v0, 2, 3\n\t" \
  "v_and_b32 v2, 3, v0\n\t" \
  "v_lshlrev_b32 v1, 7, v1\n\t" \
  "v_lshl_or_b32 v1, v2, 13, v1\n\t" \
  "v_lshrrev_b32 v2, 5, v0\n\t" \
  "v_lshlrev_b32 v2, 2, v2\n\t" \
  "v_lshrrev_b32 v2, v2, %5\n\t" \
  "v_and_b32 v3, 7, v2\n\t" \
  "v_bfe_u32 v2, v2, 8, 3\n\t" \
  "v_lshl_or_b32 v3, v3, 10, v1\n\t" \
  "v_lshl_or_b32 v2, v2, 10, v1\n\t" \
  ".Lp_%=:\n\t" \
  "buffer_load_dword v4, v2, %3, %4 offen sc1\n\t" \
  "buffer_load_dword v5, v3, %3, %4 offen sc1\n\t" \
  "s_waitcnt vmcnt(0)\n\t" \
  "v_min_u32 v4, v4, v5\n\t" \
  "v_cmp_gt_u32 vcc, 0x600, v4\n\t" \
  "s_cbranch_vccz .Lend_%=\n\t" \
  "s_add_u32 %1, %1, 1\n\t" \
  "s_cmp_gt_u32 %1, 0x100000\n\t" \
  "s_cbranch_scc1 .Lend_%=\n\t" \
  "s_sleep 3\n\t" \
  "s_branch .Lp_%=\n\t" \
  ".Lb_%=:\n\t" \
  "s_cmp_gt_u32 %2, 12\n\t" \
  "s_cbranch_scc1 .Lt_%=\n\t" \
  "s_barrier\n\t" \
  "s_branch .Lend_%=\n\t" \
  ".Lt_%=:\n\t" \
  "s_barrier\n\t" \
  "s_setprio 3\n\t" \
  "v_lshrrev_b32 v1, 3, v0\n\t" \
  "v_mul_u32_u24 v1, 100480, v1\n\t" \
  "v_and_b32 v2, 7, v0\n\t" \
  "v_lshl_add_u32 v1, v2, 4, v1\n\t" \
  "s_mul_i32 %0, %2, 28896\n\t" \
  "s_sub_u32 %0, %0, 285344\n\t" \
  "v_lshl_add_u32 v2, v0, 4, %0\n\t" \
  "s_sleep 12\n\t" \
  ".Lr_%=:\n\t" \
  "s_mov_b32 %0, %6\n\t" \
  "buffer_load_dwordx4 v[8:11], v1, %3, %0 offen sc1\n\t" \
  "s_add_u32 %0, %0, 803840\n\t" \
  "buffer_load_dwordx4 v[12:15], v1, %3, %0 offen sc1\n\t" \
  "s_add_u32 %0, %0, 803840\n\t" \
  "buffer_load_dwordx4 v[16:19], v1, %3, %0 offen sc1\n\t" \
  "s_add_u32 %0, %0, 803840\n\t" \
  "buffer_load_dwordx4 v[20:23], v1, %3, %0 offen sc1\n\t" \
  "s_waitcnt vmcnt(3)\n\t" \
  "ds_write_b128 v2, v[8:11] offset:0\n\t" \
  "v_max3_u32 v3, v8, v9, 0\n\t" \
  "v_max3_u32 v3, v10, v11, v3\n\t" \
  "s_waitcnt vmcnt(2)\n\t" \
  "ds_write_b128 v2, v[12:15] offset:1024\n\t" \
  "v_max3_u32 v3, v12, v13, v3\n\t" \
  "v_max3_u32 v3, v14, v15, v3\n\t" \
  "s_waitcnt vmcnt(1)\n\t" \
  "ds_write_b128 v2, v[16:19] offset:2048\n\t" \
  "v_max3_u32 v3, v16, v17, v3\n\t" \
  "v_max3_u32 v3, v18, v19, v3\n\t" \
  "s_waitcnt vmcnt(0)\n\t" \
  "ds_write_b128 v2, v[20:23] offset:3072\n\t" \
  "v_max3_u32 v3, v20, v21, v3\n\t" \
  "v_max3_u32 v3, v22, v23, v3\n\t" \
  "v_cmp_eq_u32 vcc, -1, v3\n\t" \
  "s_waitcnt lgkmcnt(0)\n\t" \
  "s_cbranch_vccz .Lk_%=\n\t" \
  "s_add_u32 %1, %1, 1\n\t" \
  "s_cmp_gt_u32 %1, 0x80000\n\t" \
  "s_cbranch_scc1 .Lk_%=\n\t" \
  "s_sleep 4\n\t" \
  "s_branch .Lr_%=\n\t" \
  ".Lk_%=:\n\t" \
  "s_lshl_b32 %0, %2, 2\n\t" \
  "s_add_u32 %0, %0, 153468\n\t" \
  "v_mov_b32 v1, %0\n\t" \
  "v_mov_b32 v2, 1\n\t" \
  "s_mov_b64 exec, 1\n\t" \
  "ds_add_u32 v1, v2\n\t" \
  "s_mov_b64 exec, -1\n\t" \
  ".Lend_%=:\n\t" \
  : "=&s"(T1), "=&s"(T2) : "s"(W), "s"(RSRC), "s"(FB), "s"(PP), "s"(T7) : "memory", "scc", "vcc", "v0", "v1", "v2", "v3", "v4", "v5", "v6", "v7", "v8", "v9", "v10", "v11", "v12", "v13", "v14", "v15", "v16", "v17", "v18", "v19", "v20", "v21", "v22", "v23")
#define FINAL_ASM(OUT, C12, T1, T2, SWR, W, RSRC, RSX, DIS, TM0, TSO, WSO, FO, FL, T7, ACT, FP) asm volatile( \
  "s_mov_b32 s94, 15\n\t" \
  "s_mov_b32 s95, 15\n\t" \
  "s_mov_b32 s96, 0xaaaaaaaa\n\t" \
  "s_mov_b32 s97, 0xaaaaaaaa\n\t" \
  "s_mov_b32 s98, 0xcccccccc\n\t" \
  "s_mov_b32 s99, 0xcccccccc\n\t" \
  "s_cmp_gt_u32 %5, 7\n\t" \
  "s_cbranch_scc1 .Lb_%=\n\t" \
  "s_cmp_gt_u32 %5, 1\n\t" \
  "s_cbranch_scc1 .Lq_%=\n\t" \
  "s_cmp_eq_u32 %5, 1\n\t" \
  "s_cbranch_scc1 .Lq1_%=\n\t" \
  "s_mov_b32 %2, 19292160\n\t" \
  "buffer_load_dword v40, v119, %6, %2 offen sc1\n\t" \
  "buffer_load_dword v41, v120, %6, %2 offen sc1\n\t" \
  "s_branch .Lq_%=\n\t" \
  ".Lq1_%=:\n\t" \
  "buffer_load_dword v40, v119, %7, 0 offen\n\t" \
  "buffer_load_dword v41, v120, %7, 0 offen\n\t" \
  ".Lq_%=:\n\t" \
  "s_mov_b64 exec, s[94:95]\n\t" \
  "buffer_load_dwordx4 v[0:3], v112, %7, 0 offen offset:0\n\t" \
  "buffer_load_dwordx4 v[4:7], v112, %7, 0 offen offset:32\n\t" \
  "buffer_load_dwordx4 v[8:11], v112, %7, 0 offen offset:64\n\t" \
  "buffer_load_dwordx4 v[12:15], v112, %7, 0 offen offset:96\n\t" \
  "buffer_load_dwordx4 v[16:19], v112, %7, 0 offen offset:128\n\t" \
  "buffer_load_dwordx4 v[20:23], v112, %7, 0 offen offset:160\n\t" \
  "buffer_load_dwordx4 v[24:27], v112, %7, 0 offen offset:192\n\t" \
  "buffer_load_dwordx4 v[28:31], v112, %7, 0 offen offset:224\n\t" \
  "s_mov_b64 exec, -1\n\t" \
  "global_load_ushort v120, v117, %8\n\t" \
  "s_barrier\n\t" \
  "s_cmp_lt_u32 %5, 4\n\t" \
  "s_cbranch_scc1 .Ltk_%=\n\t" \
  "s_lshl_b32 %2, %5, 13\n\t" \
  "s_mov_b32 m0, %2\n\t" \
  "s_add_u32 %2, %2, %11\n\t" \
  "buffer_load_dwordx4 v121, %6, %2 offen offset:0 sc1 lds\n\t" \
  "buffer_load_dwordx4 v121, %6, %2 offen offset:1024 sc1 lds\n\t" \
  "buffer_load_dwordx4 v121, %6, %2 offen offset:2048 sc1 lds\n\t" \
  "buffer_load_dwordx4 v121, %6, %2 offen offset:3072 sc1 lds\n\t" \
  "s_add_u32 m0, m0, 4096\n\t" \
  "s_add_u32 %2, %2, 4096\n\t" \
  "buffer_load_dwordx4 v121, %6, %2 offen offset:0 sc1 lds\n\t" \
  "buffer_load_dwordx4 v121, %6, %2 offen offset:1024 sc1 lds\n\t" \
  "buffer_load_dwordx4 v121, %6, %2 offen offset:2048 sc1 lds\n\t" \
  "buffer_load_dwordx4 v121, %6, %2 offen offset:3072 sc1 lds\n\t" \
  "s_branch .Ltk_%=\n\t" \
  ".Lb_%=:\n\t" \
  "s_cmp_gt_u32 %5, 11\n\t" \
  "s_cbranch_scc1 .Lc_%=\n\t" \
  "s_mov_b64 exec, s[94:95]\n\t" \
  "buffer_load_dwordx4 v[0:3], v112, %7, 0 offen offset:0\n\t" \
  "buffer_load_dwordx4 v[4:7], v112, %7, 0 offen offset:32\n\t" \
  "buffer_load_dwordx4 v[8:11], v112, %7, 0 offen offset:64\n\t" \
  "buffer_load_dwordx4 v[12:15], v112, %7, 0 offen offset:96\n\t" \
  "buffer_load_dwordx4 v[16:19], v112, %7, 0 offen offset:128\n\t" \
  "buffer_load_dwordx4 v[20:23], v112, %7, 0 offen offset:160\n\t" \
  "buffer_load_dwordx4 v[24:27], v112, %7, 0 offen offset:192\n\t" \
  "buffer_load_dwordx4 v[28:31], v112, %7, 0 offen offset:224\n\t" \
  "s_mov_b64 exec, -1\n\t" \
  "global_load_ushort v120, v117, %8\n\t" \
  ".Ltk_%=:\n\t" \
  "s_mov_b32 m0, %9\n\t" \
  "s_mov_b32 %2, %10\n\t" \
  "buffer_load_dwordx4 v113, %6, %2 offen sc1 lds\n\t" \
  "s_add_u32 m0, m0, 4128\n\t" \
  "s_add_u32 %2, %2, 3215360\n\t" \
  "buffer_load_dwordx4 v113, %6, %2 offen sc1 lds\n\t" \
  "s_add_u32 m0, m0, 4128\n\t" \
  "s_add_u32 %2, %2, 3215360\n\t" \
  "buffer_load_dwordx4 v113, %6, %2 offen sc1 lds\n\t" \
  "s_add_u32 m0, m0, 4128\n\t" \
  "s_add_u32 %2, %2, 3215360\n\t" \
  "buffer_load_dwordx4 v113, %6, %2 offen sc1 lds\n\t" \
  "s_add_u32 m0, m0, 4128\n\t" \
  "s_add_u32 %2, %2, 3215360\n\t" \
  "buffer_load_dwordx4 v113, %6, %2 offen sc1 lds\n\t" \
  "s_add_u32 m0, m0, 4128\n\t" \
  "s_add_u32 %2, %2, 3215360\n\t" \
  "buffer_load_dwordx4 v113, %6, %2 offen sc1 lds\n\t" \
  "s_and_b32 %2, %5, 12\n\t" \
  "s_add_u32 %2, %2, 153520\n\t" \
  "v_mov_b32 v32, %2\n\t" \
  "v_mov_b32 v33, 1\n\t" \
  "s_mov_b64 exec, 1\n\t" \
  "s_lshr_b32 %2, %5, 2\n\t" \
  "s_cmp_lg_u32 %2, 1\n\t" \
  "s_cbranch_scc1 .Lnw_%=\n\t" \
  "s_waitcnt vmcnt(6)\n\t" \
  "ds_add_u32 v32, v33 offset:8\n\t" \
  ".Lnw_%=:\n\t" \
  "s_waitcnt vmcnt(0)\n\t" \
  "ds_add_u32 v32, v33\n\t" \
  "s_mov_b64 exec, -1\n\t" \
  ".Lw_%=:\n\t" \
  "ds_read_b32 v33, v32\n\t" \
  "s_waitcnt lgkmcnt(0)\n\t" \
  "v_cmp_gt_u32 vcc, 5, v33\n\t" \
  "s_cbranch_vccz .Lc_%=\n\t" \
  "s_sleep 1\n\t" \
  "s_branch .Lw_%=\n\t" \
  ".Lc_%=:\n\t" \
  "s_cmp_le_i32 %15, 0\n\t" \
  "s_cbranch_scc1 .Lend_%=\n\t" \
  "s_cmp_gt_u32 %5, 11\n\t" \
  "s_cbranch_scc1 .Lh_%=\n\t" \
  "v_mov_b32 v32, 153504\n\t" \
  "ds_read_b32 v32, v32\n\t" \
  "s_not_b64 exec, s[94:95]\n\t" \
  "ds_read_b64 v[0:1], v116 offset:0\n\t" \
  "ds_read_b64 v[2:3], v116 offset:128\n\t" \
  "ds_read_b64 v[4:5], v116 offset:512\n\t" \
  "ds_read_b64 v[6:7], v116 offset:640\n\t" \
  "ds_read_b64 v[8:9], v116 offset:1024\n\t" \
  "ds_read_b64 v[10:11], v116 offset:1152\n\t" \
  "ds_read_b64 v[12:13], v116 offset:1536\n\t" \
  "ds_read_b64 v[14:15], v116 offset:1664\n\t" \
  "ds_read_b64 v[16:17], v116 offset:2048\n\t" \
  "ds_read_b64 v[18:19], v116 offset:2176\n\t" \
  "ds_read_b64 v[20:21], v116 offset:2560\n\t" \
  "ds_read_b64 v[22:23], v116 offset:2688\n\t" \
  "ds_read_b64 v[24:25], v116 offset:3072\n\t" \
  "ds_read_b64 v[26:27], v116 offset:3200\n\t" \
  "ds_read_b64 v[28:29], v116 offset:3584\n\t" \
  "ds_read_b64 v[30:31], v116 offset:3712\n\t" \
  "s_mov_b64 exec, -1\n\t" \
  "v_lshlrev_b32 v120, 16, v120\n\t" \
  "v_cmp_eq_f32 vcc, 0, v120\n\t" \
  "s_or_b64 %3, vcc, s[94:95]\n\t" \
  "v_cndmask_b32 v120, v120, 1.0, %3\n\t" \
  "s_waitcnt vmcnt(0) lgkmcnt(0)\n\t" \
  "v_readfirstlane_b32 %4, v32\n\t" \
  "s_cmp_gt_u32 %5, 3\n\t" \
  "s_cbranch_scc1 .Lg_%=\n\t" \
  "v_mov_b32 v33, 153540\n\t" \
  "v_mov_b32 v34, 1\n\t" \
  "s_mov_b64 exec, 1\n\t" \
  "ds_add_u32 v33, v34\n\t" \
  "s_mov_b64 exec, -1\n\t" \
  ".Lg_%=:\n\t" \
  "s_setprio 1\n\t" \
  "ds_read_b128 v[96:99], v121 offset:0\n\t" \
  "ds_read_b128 v[100:103], v121 offset:1024\n\t" \
  "ds_read_b128 v[104:107], v121 offset:2048\n\t" \
  "ds_read_b128 v[108:111], v121 offset:3072\n\t" \
  "ds_read_b128 v[48:51], v118 offset:0\n\t" \
  "ds_read_b128 v[52:55], v118 offset:32\n\t" \
  "ds_read_b128 v[56:59], v118 offset:64\n\t" \
  "ds_read_b128 v[60:63], v118 offset:96\n\t" \
  "ds_read_b128 v[64:67], v118 offset:128\n\t" \
  "ds_read_b128 v[68:71], v118 offset:160\n\t" \
  "ds_read_b128 v[72:75], v118 offset:192\n\t" \
  "ds_read_b128 v[76:79], v118 offset:224\n\t" \
  "s_waitcnt lgkmcnt(11)\n\t" \
  "v_mfma_f32_32x32x16_bf16 v[32:47], v[96:99], v[0:3], 0\n\t" \
  "ds_read_b128 v[96:99], v121 offset:4096\n\t" \
  "v_mfma_f32_4x4x4_16b_bf16 v[88:91], v[0:1], v[0:1], 0\n\t" \
  "v_mfma_f32_4x4x4_16b_bf16 v[92:95], v[2:3], v[2:3], 0\n\t" \
  "s_waitcnt lgkmcnt(8)\n\t" \
  "v_mfma_f32_16x16x32_bf16 v[112:115], v[48:51], v[0:3], 0\n\t" \
  "v_mfma_f32_32x32x16_bf16 v[32:47], v[100:103], v[4:7], v[32:47]\n\t" \
  "ds_read_b128 v[100:103], v121 offset:5120\n\t" \
  "v_mfma_f32_4x4x4_16b_bf16 v[88:91], v[4:5], v[4:5], v[88:91]\n\t" \
  "v_mfma_f32_4x4x4_16b_bf16 v[92:95], v[6:7], v[6:7], v[92:95]\n\t" \
  "s_waitcnt lgkmcnt(8)\n\t" \
  "v_mfma_f32_16x16x32_bf16 v[112:115], v[52:55], v[4:7], v[112:115]\n\t" \
  "v_mfma_f32_32x32x16_bf16 v[32:47], v[104:107], v[8:11], v[32:47]\n\t" \
  "ds_read_b128 v[104:107], v121 offset:6144\n\t" \
  "v_mfma_f32_4x4x4_16b_bf16 v[88:91], v[8:9], v[8:9], v[88:91]\n\t" \
  "v_mfma_f32_4x4x4_16b_bf16 v[92:95], v[10:11], v[10:11], v[92:95]\n\t" \
  "s_waitcnt lgkmcnt(8)\n\t" \
  "v_mfma_f32_16x16x32_bf16 v[112:115], v[56:59], v[8:11], v[112:115]\n\t" \
  "v_mfma_f32_32x32x16_bf16 v[32:47], v[108:111], v[12:15], v[32:47]\n\t" \
  "ds_read_b128 v[108:111], v121 offset:7168\n\t" \
  "v_mfma_f32_4x4x4_16b_bf16 v[88:91], v[12:13], v[12:13], v[88:91]\n\t" \
  "v_mfma_f32_4x4x4_16b_bf16 v[92:95], v[14:15], v[14:15], v[92:95]\n\t" \
  "s_waitcnt lgkmcnt(8)\n\t" \
  "v_mfma_f32_16x16x32_bf16 v[112:115], v[60:63], v[12:15], v[112:115]\n\t" \
  "s_waitcnt lgkmcnt(3)\n\t" \
  "v_mfma_f32_32x32x16_bf16 v[32:47], v[96:99], v[16:19], v[32:47]\n\t" \
  "ds_read_b128 v[96:99], v121 offset:8192\n\t" \
  "v_mfma_f32_4x4x4_16b_bf16 v[88:91], v[16:17], v[16:17], v[88:91]\n\t" \
  "v_mfma_f32_4x4x4_16b_bf16 v[92:95], v[18:19], v[18:19], v[92:95]\n\t" \
  "v_mfma_f32_16x16x32_bf16 v[112:115], v[64:67], v[16:19], v[112:115]\n\t" \
  "s_waitcnt lgkmcnt(3)\n\t" \
  "v_mfma_f32_32x32x16_bf16 v[32:47], v[100:103], v[20:23], v[32:47]\n\t" \
  "ds_read_b128 v[100:103], v121 offset:9216\n\t" \
  "v_mfma_f32_4x4x4_16b_bf16 v[88:91], v[20:21], v[20:21], v[88:91]\n\t" \
  "v_mfma_f32_4x4x4_16b_bf16 v[92:95], v[22:23], v[22:23], v[92:95]\n\t" \
  "v_mfma_f32_16x16x32_bf16 v[112:115], v[68:71], v[20:23], v[112:115]\n\t" \
  "s_waitcnt lgkmcnt(3)\n\t" \
  "v_mfma_f32_32x32x16_bf16 v[32:47], v[104:107], v[24:27], v[32:47]\n\t" \
  "ds_read_b128 v[104:107], v121 offset:10240\n\t" \
  "v_mfma_f32_4x4x4_16b_bf16 v[88:91], v[24:25], v[24:25], v[88:91]\n\t" \
  "v_mfma_f32_4x4x4_16b_bf16 v[92:95], v[26:27], v[26:27], v[92:95]\n\t" \
  "v_mfma_f32_16x16x32_bf16 v[112:115], v[72:75], v[24:27], v[112:115]\n\t" \
  "s_waitcnt lgkmcnt(3)\n\t" \
  "v_mfma_f32_32x32x16_bf16 v[32:47], v[108:111], v[28:31], v[32:47]\n\t" \
  "ds_read_b128 v[108:111], v121 offset:11264\n\t" \
  "v_mfma_f32_4x4x4_16b_bf16 v[88:91], v[28:29], v[28:29], v[88:91]\n\t" \
  "v_mfma_f32_4x4x4_16b_bf16 v[92:95], v[30:31], v[30:31], v[92:95]\n\t" \
  "v_mfma_f32_16x16x32_bf16 v[112:115], v[76:79], v[28:31], v[112:115]\n\t" \
  "s_waitcnt lgkmcnt(3)\n\t" \
  "v_mfma_f32_32x32x16_bf16 v[48:63], v[96:99], v[0:3], 0\n\t" \
  "ds_read_b128 v[96:99], v121 offset:12288\n\t" \
  "s_waitcnt lgkmcnt(3)\n\t" \
  "v_mfma_f32_32x32x16_bf16 v[48:63], v[100:103], v[4:7], v[48:63]\n\t" \
  "ds_read_b128 v[100:103], v121 offset:13312\n\t" \
  "s_waitcnt lgkmcnt(3)\n\t" \
  "v_mfma_f32_32x32x16_bf16 v[48:63], v[104:107], v[8:11], v[48:63]\n\t" \
  "ds_read_b128 v[104:107], v121 offset:14336\n\t" \
  "v_cvt_pk_bf16_f32 v80, v32, v33\n\t" \
  "v_cvt_pk_bf16_f32 v81, v34, v35\n\t" \
  "v_cvt_pk_bf16_f32 v82, v36, v37\n\t" \
  "v_cvt_pk_bf16_f32 v83, v38, v39\n\t" \
  "s_waitcnt lgkmcnt(3)\n\t" \
  "v_mfma_f32_32x32x16_bf16 v[48:63], v[108:111], v[12:15], v[48:63]\n\t" \
  "ds_read_b128 v[108:111], v121 offset:15360\n\t" \
  "v_cvt_pk_bf16_f32 v84, v40, v41\n\t" \
  "v_cvt_pk_bf16_f32 v85, v42, v43\n\t" \
  "v_cvt_pk_bf16_f32 v86, v44, v45\n\t" \
  "v_cvt_pk_bf16_f32 v87, v46, v47\n\t" \
  "s_waitcnt lgkmcnt(3)\n\t" \
  "v_mfma_f32_32x32x16_bf16 v[48:63], v[96:99], v[16:19], v[48:63]\n\t" \
  "ds_read_b128 v[96:99], v121 offset:16384\n\t" \
  "v_add_f32 v88, v88, v92\n\t" \
  "v_add_f32 v89, v89, v93\n\t" \
  "v_add_f32 v90, v90, v94\n\t" \
  "v_add_f32 v91, v91, v95\n\t" \
  "v_mfma_f32_32x32x16_bf16 v[64:79], v[0:3], v[80:83], 0\n\t" \
  "s_waitcnt lgkmcnt(3)\n\t" \
  "v_mfma_f32_32x32x16_bf16 v[48:63], v[100:103], v[20:23], v[48:63]\n\t" \
  "ds_read_b128 v[100:103], v121 offset:17408\n\t" \
  "v_cndmask_b32 v88, v88, v89, s[96:97]\n\t" \
  "v_cndmask_b32 v90, v90, v91, s[96:97]\n\t" \
  "v_cndmask_b32 v124, v88, v90, s[98:99]\n\t" \
  "v_rcp_f32 v90, v120\n\t" \
  "v_mfma_f32_32x32x16_bf16 v[64:79], v[4:7], v[84:87], v[64:79]\n\t" \
  "s_waitcnt lgkmcnt(3)\n\t" \
  "v_mfma_f32_32x32x16_bf16 v[48:63], v[104:107], v[24:27], v[48:63]\n\t" \
  "ds_read_b128 v[104:107], v121 offset:18432\n\t" \
  "v_mov_b32 v88, v124\n\t" \
  "v_mul_f32 v91, v90, v90\n\t" \
  "v_mul_f32 v89, 0x3c000000, v112\n\t" \
  "v_permlane32_swap_b32 v124, v88\n\t" \
  "s_waitcnt lgkmcnt(3)\n\t" \
  "v_mfma_f32_32x32x16_bf16 v[48:63], v[108:111], v[28:31], v[48:63]\n\t" \
  "ds_read_b128 v[108:111], v121 offset:19456\n\t" \
  "v_add_f32 v88, v124, v88\n\t" \
  "v_mul_f32 v88, 0x3c000000, v88\n\t" \
  "v_fma_f32 v88, -v89, v89, v88\n\t" \
  "v_max_f32 v88, 0, v88\n\t" \
  "s_waitcnt lgkmcnt(3)\n\t" \
  "v_mfma_f32_32x32x16_bf16 v[32:47], v[96:99], v[0:3], 0\n\t" \
  "ds_read_b128 v[96:99], v121 offset:20480\n\t" \
  "v_fmaak_f32 v88, v91, v88, 0x3727c5ac\n\t" \
  "v_rsq_f32 v88, v88\n\t" \
  "v_mov_b32 v124, v113\n\t" \
  "v_mul_f32 v113, v90, v88\n\t" \
  "s_waitcnt lgkmcnt(3)\n\t" \
  "v_mfma_f32_32x32x16_bf16 v[32:47], v[100:103], v[4:7], v[32:47]\n\t" \
  "ds_read_b128 v[100:103], v121 offset:21504\n\t" \
  "ds_write_b128 v122, v[112:115]\n\t" \
  "s_waitcnt lgkmcnt(4)\n\t" \
  "v_mfma_f32_32x32x16_bf16 v[32:47], v[104:107], v[8:11], v[32:47]\n\t" \
  "ds_read_b128 v[104:107], v121 offset:22528\n\t" \
  "v_cvt_pk_bf16_f32 v80, v48, v49\n\t" \
  "v_cvt_pk_bf16_f32 v81, v50, v51\n\t" \
  "v_cvt_pk_bf16_f32 v82, v52, v53\n\t" \
  "v_cvt_pk_bf16_f32 v83, v54, v55\n\t" \
  "s_waitcnt lgkmcnt(4)\n\t" \
  "v_mfma_f32_32x32x16_bf16 v[32:47], v[108:111], v[12:15], v[32:47]\n\t" \
  "ds_read_b128 v[108:111], v121 offset:23552\n\t" \
  "v_cvt_pk_bf16_f32 v84, v56, v57\n\t" \
  "v_cvt_pk_bf16_f32 v85, v58, v59\n\t" \
  "v_cvt_pk_bf16_f32 v86, v60, v61\n\t" \
  "v_cvt_pk_bf16_f32 v87, v62, v63\n\t" \
  "s_waitcnt lgkmcnt(4)\n\t" \
  "v_mfma_f32_32x32x16_bf16 v[32:47], v[96:99], v[16:19], v[32:47]\n\t" \
  "ds_read_b128 v[96:99], v121 offset:24576\n\t" \
  "v_mul_f32 v113, 0x3e0293ee, v113\n\t" \
  "v_mov_b32 v114, v90\n\t" \
  "v_mov_b32 v115, v91\n\t" \
  "v_mfma_f32_32x32x16_bf16 v[64:79], v[8:11], v[80:83], v[64:79]\n\t" \
  "s_waitcnt lgkmcnt(4)\n\t" \
  "v_mfma_f32_32x32x16_bf16 v[32:47], v[100:103], v[20:23], v[32:47]\n\t" \
  "ds_read_b128 v[100:103], v121 offset:25600\n\t" \
  "v_mfma_f32_32x32x16_bf16 v[64:79], v[12:15], v[84:87], v[64:79]\n\t" \
  "s_waitcnt lgkmcnt(3)\n\t" \
  "v_mfma_f32_32x32x16_bf16 v[32:47], v[104:107], v[24:27], v[32:47]\n\t" \
  "ds_read_b128 v[104:107], v121 offset:26624\n\t" \
  "s_waitcnt lgkmcnt(3)\n\t" \
  "v_mfma_f32_32x32x16_bf16 v[32:47], v[108:111], v[28:31], v[32:47]\n\t" \
  "ds_read_b128 v[108:111], v121 offset:27648\n\t" \
  "s_waitcnt lgkmcnt(3)\n\t" \
  "v_mfma_f32_32x32x16_bf16 v[48:63], v[96:99], v[0:3], 0\n\t" \
  "ds_read_b128 v[96:99], v121 offset:28672\n\t" \
  "s_waitcnt lgkmcnt(3)\n\t" \
  "v_mfma_f32_32x32x16_bf16 v[48:63], v[100:103], v[4:7], v[48:63]\n\t" \
  "ds_read_b128 v[100:103], v121 offset:29696\n\t" \
  "s_waitcnt lgkmcnt(3)\n\t" \
  "v_mfma_f32_32x32x16_bf16 v[48:63], v[104:107], v[8:11], v[48:63]\n\t" \
  "ds_read_b128 v[104:107], v121 offset:30720\n\t" \
  "s_nop 1\n\t" \
  "v_cvt_pk_bf16_f32 v80, v32, v33\n\t" \
  "v_cvt_pk_bf16_f32 v81, v34, v35\n\t" \
  "v_cvt_pk_bf16_f32 v82, v36, v37\n\t" \
  "v_cvt_pk_bf16_f32 v83, v38, v39\n\t" \
  "s_waitcnt lgkmcnt(3)\n\t" \
  "v_mfma_f32_32x32x16_bf16 v[48:63], v[108:111], v[12:15], v[48:63]\n\t" \
  "ds_read_b128 v[108:111], v121 offset:31744\n\t" \
  "v_cvt_pk_bf16_f32 v84, v40, v41\n\t" \
  "v_cvt_pk_bf16_f32 v85, v42, v43\n\t" \
  "v_cvt_pk_bf16_f32 v86, v44, v45\n\t" \
  "v_cvt_pk_bf16_f32 v87, v46, v47\n\t" \
  "s_waitcnt lgkmcnt(3)\n\t" \
  "v_mfma_f32_32x32x16_bf16 v[48:63], v[96:99], v[16:19], v[48:63]\n\t" \
  "v_mov_b32 v125, 153532\n\t" \
  ".Lwc_%=:\n\t" \
  "ds_read_b32 v116, v125\n\t" \
  "s_waitcnt lgkmcnt(0)\n\t" \
  "v_cmp_gt_u32 vcc, 4, v116\n\t" \
  "s_cbranch_vccnz .Lwc_%=\n\t" \
  "ds_read_b128 v[96:99], v121 offset:32768\n\t" \
  "v_mfma_f32_32x32x16_bf16 v[64:79], v[16:19], v[80:83], v[64:79]\n\t" \
  "v_mfma_f32_32x32x16_bf16 v[48:63], v[100:103], v[20:23], v[48:63]\n\t" \
  "ds_read_b128 v[100:103], v121 offset:33792\n\t" \
  "v_mfma_f32_32x32x16_bf16 v[64:79], v[20:23], v[84:87], v[64:79]\n\t" \
  "v_mfma_f32_32x32x16_bf16 v[48:63], v[104:107], v[24:27], v[48:63]\n\t" \
  "ds_read_b128 v[104:107], v121 offset:34816\n\t" \
  "v_mfma_f32_32x32x16_bf16 v[48:63], v[108:111], v[28:31], v[48:63]\n\t" \
  "ds_read_b128 v[108:111], v121 offset:35840\n\t" \
  "s_waitcnt lgkmcnt(3)\n\t" \
  "v_mfma_f32_32x32x16_bf16 v[32:47], v[0:3], v[96:99], 0\n\t" \
  "ds_read_b128 v[96:99], v121 offset:36864\n\t" \
  "s_waitcnt lgkmcnt(3)\n\t" \
  "v_mfma_f32_32x32x16_bf16 v[32:47], v[4:7], v[100:103], v[32:47]\n\t" \
  "ds_read_b128 v[100:103], v121 offset:37888\n\t" \
  "s_waitcnt lgkmcnt(3)\n\t" \
  "v_mfma_f32_32x32x16_bf16 v[32:47], v[8:11], v[104:107], v[32:47]\n\t" \
  "ds_read_b128 v[104:107], v121 offset:38912\n\t" \
  "s_nop 1\n\t" \
  "v_cvt_pk_bf16_f32 v80, v48, v49\n\t" \
  "v_cvt_pk_bf16_f32 v81, v50, v51\n\t" \
  "v_cvt_pk_bf16_f32 v82, v52, v53\n\t" \
  "v_cvt_pk_bf16_f32 v83, v54, v55\n\t" \
  "s_waitcnt lgkmcnt(3)\n\t" \
  "v_mfma_f32_32x32x16_bf16 v[32:47], v[12:15], v[108:111], v[32:47]\n\t" \
  "ds_read_b128 v[108:111], v121 offset:39936\n\t" \
  "v_cvt_pk_bf16_f32 v84, v56, v57\n\t" \
  "v_cvt_pk_bf16_f32 v85, v58, v59\n\t" \
  "v_cvt_pk_bf16_f32 v86, v60, v61\n\t" \
  "v_cvt_pk_bf16_f32 v87, v62, v63\n\t" \
  "s_waitcnt lgkmcnt(3)\n\t" \
  "v_mfma_f32_32x32x16_bf16 v[32:47], v[16:19], v[96:99], v[32:47]\n\t" \
  "ds_read_b128 v[96:99], v121 offset:40960\n\t" \
  "v_mfma_f32_32x32x16_bf16 v[64:79], v[24:27], v[80:83], v[64:79]\n\t" \
  "s_waitcnt lgkmcnt(3)\n\t" \
  "v_mfma_f32_32x32x16_bf16 v[32:47], v[20:23], v[100:103], v[32:47]\n\t" \
  "ds_read_b128 v[100:103], v121 offset:41984\n\t" \
  "v_mfma_f32_32x32x16_bf16 v[64:79], v[28:31], v[84:87], v[64:79]\n\t" \
  "s_waitcnt lgkmcnt(3)\n\t" \
  "v_mfma_f32_32x32x16_bf16 v[32:47], v[24:27], v[104:107], v[32:47]\n\t" \
  "ds_read_b128 v[104:107], v121 offset:43008\n\t" \
  "s_waitcnt lgkmcnt(3)\n\t" \
  "v_mfma_f32_32x32x16_bf16 v[32:47], v[28:31], v[108:111], v[32:47]\n\t" \
  "ds_read_b128 v[108:111], v121 offset:44032\n\t" \
  "s_waitcnt lgkmcnt(3)\n\t" \
  "v_mfma_f32_32x32x16_bf16 v[48:63], v[0:3], v[96:99], 0\n\t" \
  "ds_read_b128 v[96:99], v121 offset:45056\n\t" \
  "s_nop 2\n\t" \
  "v_cndmask_b32 v64, v64, v65, s[96:97]\n\t" \
  "v_cndmask_b32 v66, v66, v67, s[96:97]\n\t" \
  "v_cndmask_b32 v68, v68, v69, s[96:97]\n\t" \
  "v_cndmask_b32 v70, v70, v71, s[96:97]\n\t" \
  "v_cndmask_b32 v72, v72, v73, s[96:97]\n\t" \
  "s_waitcnt lgkmcnt(3)\n\t" \
  "v_mfma_f32_32x32x16_bf16 v[48:63], v[4:7], v[100:103], v[48:63]\n\t" \
  "ds_read_b128 v[100:103], v121 offset:46080\n\t" \
  "v_cndmask_b32 v74, v74, v75, s[96:97]\n\t" \
  "v_cndmask_b32 v76, v76, v77, s[96:97]\n\t" \
  "v_cndmask_b32 v78, v78, v79, s[96:97]\n\t" \
  "v_cndmask_b32 v116, v64, v66, s[98:99]\n\t" \
  "v_cndmask_b32 v117, v68, v70, s[98:99]\n\t" \
  "s_waitcnt lgkmcnt(3)\n\t" \
  "v_mfma_f32_32x32x16_bf16 v[48:63], v[8:11], v[104:107], v[48:63]\n\t" \
  "ds_read_b128 v[104:107], v121 offset:47104\n\t" \
  "v_cvt_pk_bf16_f32 v80, v32, v33\n\t" \
  "v_cvt_pk_bf16_f32 v81, v34, v35\n\t" \
  "v_cvt_pk_bf16_f32 v82, v36, v37\n\t" \
  "v_cvt_pk_bf16_f32 v83, v38, v39\n\t" \
  "s_waitcnt lgkmcnt(3)\n\t" \
  "v_mfma_f32_32x32x16_bf16 v[48:63], v[12:15], v[108:111], v[48:63]\n\t" \
  "ds_read_b128 v[108:111], v121 offset:48128\n\t" \
  "v_cvt_pk_bf16_f32 v84, v40, v41\n\t" \
  "v_cvt_pk_bf16_f32 v85, v42, v43\n\t" \
  "v_cvt_pk_bf16_f32 v86, v44, v45\n\t" \
  "v_cvt_pk_bf16_f32 v87, v46, v47\n\t" \
  "s_waitcnt lgkmcnt(3)\n\t" \
  "v_mfma_f32_32x32x16_bf16 v[48:63], v[16:19], v[96:99], v[48:63]\n\t" \
  "ds_read_b128 v[96:99], v121 offset:49152\n\t" \
  "v_cndmask_b32 v118, v72, v74, s[98:99]\n\t" \
  "v_cndmask_b32 v119, v76, v78, s[98:99]\n\t" \
  "ds_read_b128 v[64:67], v123 offset:0\n\t" \
  "ds_read_b128 v[68:71], v123 offset:128\n\t" \
  "ds_read_b128 v[72:75], v123 offset:256\n\t" \
  "ds_read_b128 v[76:79], v123 offset:384\n\t" \
  "s_waitcnt lgkmcnt(7)\n\t" \
  "v_mfma_f32_32x32x16_bf16 v[48:63], v[20:23], v[100:103], v[48:63]\n\t" \
  "ds_read_b128 v[100:103], v121 offset:50176\n\t" \
  "v_mul_f32 v116, v113, v116\n\t" \
  "v_mul_f32 v117, v113, v117\n\t" \
  "v_mul_f32 v118, v113, v118\n\t" \
  "v_mul_f32 v119, v113, v119\n\t" \
  "s_waitcnt lgkmcnt(7)\n\t" \
  "v_mfma_f32_32x32x16_bf16 v[48:63], v[24:27], v[104:107], v[48:63]\n\t" \
  "ds_read_b128 v[104:107], v121 offset:51200\n\t" \
  "s_waitcnt lgkmcnt(5)\n\t" \
  "v_mul_f32 v116, v65, v116\n\t" \
  "s_waitcnt lgkmcnt(4)\n\t" \
  "v_mul_f32 v117, v69, v117\n\t" \
  "s_waitcnt lgkmcnt(3)\n\t" \
  "v_mul_f32 v118, v73, v118\n\t" \
  "s_waitcnt lgkmcnt(2)\n\t" \
  "v_mul_f32 v119, v77, v119\n\t" \
  "v_mfma_f32_32x32x16_bf16 v[48:63], v[28:31], v[108:111], v[48:63]\n\t" \
  "ds_read_b128 v[108:111], v121 offset:52224\n\t" \
  "v_max3_f32 v64, v116, v117, v118\n\t" \
  "v_max_f32 v64, v64, v119\n\t" \
  "v_mov_b32 v68, v64\n\t" \
  "v_mfma_f32_32x32x16_bf16 v[32:47], v[0:3], v[96:99], 0\n\t" \
  "ds_read_b128 v[96:99], v121 offset:53248\n\t" \
  "v_permlane32_swap_b32 v64, v68\n\t" \
  "v_max_f32 v64, v64, v68\n\t" \
  "v_sub_f32 v116, v116, v64\n\t" \
  "v_sub_f32 v117, v117, v64\n\t" \
  "v_sub_f32 v118, v118, v64\n\t" \
  "v_sub_f32 v119, v119, v64\n\t" \
  "s_waitcnt lgkmcnt(3)\n\t" \
  "v_mfma_f32_32x32x16_bf16 v[32:47], v[4:7], v[100:103], v[32:47]\n\t" \
  "ds_read_b128 v[100:103], v121 offset:54272\n\t" \
  "v_exp_f32 v116, v116\n\t" \
  "v_exp_f32 v117, v117\n\t" \
  "v_exp_f32 v118, v118\n\t" \
  "s_waitcnt lgkmcnt(3)\n\t" \
  "v_mfma_f32_32x32x16_bf16 v[32:47], v[8:11], v[104:107], v[32:47]\n\t" \
  "ds_read_b128 v[104:107], v121 offset:55296\n\t" \
  "v_cvt_pk_bf16_f32 v88, v48, v49\n\t" \
  "v_cvt_pk_bf16_f32 v89, v50, v51\n\t" \
  "v_cvt_pk_bf16_f32 v90, v52, v53\n\t" \
  "v_cvt_pk_bf16_f32 v91, v54, v55\n\t" \
  "v_exp_f32 v119, v119\n\t" \
  "s_waitcnt lgkmcnt(3)\n\t" \
  "v_mfma_f32_32x32x16_bf16 v[32:47], v[12:15], v[108:111], v[32:47]\n\t" \
  "ds_read_b128 v[108:111], v121 offset:56320\n\t" \
  "v_cvt_pk_bf16_f32 v92, v56, v57\n\t" \
  "v_cvt_pk_bf16_f32 v93, v58, v59\n\t" \
  "v_cvt_pk_bf16_f32 v94, v60, v61\n\t" \
  "v_cvt_pk_bf16_f32 v95, v62, v63\n\t" \
  "v_add_f32 v64, v116, v117\n\t" \
  "v_add_f32 v68, v118, v119\n\t" \
  "s_waitcnt lgkmcnt(3)\n\t" \
  "v_mfma_f32_32x32x16_bf16 v[32:47], v[16:19], v[96:99], v[32:47]\n\t" \
  "ds_read_b128 v[96:99], v121 offset:57344\n\t" \
  "v_add_f32 v64, v64, v68\n\t" \
  "v_mov_b32 v68, v64\n\t" \
  "v_mul_f32 v116, v65, v116\n\t" \
  "v_mul_f32 v117, v69, v117\n\t" \
  "v_mul_f32 v118, v73, v118\n\t" \
  "v_mul_f32 v119, v77, v119\n\t" \
  "s_waitcnt lgkmcnt(3)\n\t" \
  "v_mfma_f32_32x32x16_bf16 v[32:47], v[20:23], v[100:103], v[32:47]\n\t" \
  "ds_read_b128 v[100:103], v121 offset:58368\n\t" \
  "v_permlane32_swap_b32 v64, v68\n\t" \
  "v_add_f32 v64, v64, v68\n\t" \
  "v_rcp_f32 v64, v64\n\t" \
  "v_mul_f32 v72, v116, v66\n\t" \
  "v_mul_f32 v64, v120, v64\n\t" \
  "v_mul_f32 v76, v116, v67\n\t" \
  "s_waitcnt lgkmcnt(3)\n\t" \
  "v_mfma_f32_32x32x16_bf16 v[32:47], v[24:27], v[104:107], v[32:47]\n\t" \
  "ds_read_b128 v[104:107], v121 offset:59392\n\t" \
  "v_fmac_f32 v72, v117, v70\n\t" \
  "v_fmac_f32 v76, v117, v71\n\t" \
  "v_fmac_f32 v72, v118, v74\n\t" \
  "v_fmac_f32 v76, v118, v75\n\t" \
  "v_fmac_f32 v72, v119, v78\n\t" \
  "v_fmac_f32 v76, v119, v79\n\t" \
  "s_waitcnt lgkmcnt(3)\n\t" \
  "v_mfma_f32_32x32x16_bf16 v[32:47], v[28:31], v[108:111], v[32:47]\n\t" \
  "ds_read_b128 v[108:111], v121 offset:60416\n\t" \
  "v_mul_f32 v116, v64, v116\n\t" \
  "v_mul_f32 v117, v64, v117\n\t" \
  "v_mul_f32 v118, v64, v118\n\t" \
  "v_mul_f32 v119, v64, v119\n\t" \
  "v_permlane32_swap_b32 v72, v76\n\t" \
  "v_add_f32 v72, v72, v76\n\t" \
  "s_waitcnt lgkmcnt(3)\n\t" \
  "v_mfma_f32_32x32x16_bf16 v[48:63], v[0:3], v[96:99], 0\n\t" \
  "ds_read_b128 v[96:99], v121 offset:61440\n\t" \
  "v_mul_f32 v72, v64, v72\n\t" \
  "v_mov_b32 v77, 0xffff\n\t" \
  "v_mov_b32 v76, v72\n\t" \
  "v_not_b32 v78, v77\n\t" \
  "v_cndmask_b32 v77, v77, v78, s[96:97]\n\t" \
  "v_permlane32_swap_b32 v72, v76\n\t" \
  "s_waitcnt lgkmcnt(3)\n\t" \
  "v_mfma_f32_32x32x16_bf16 v[48:63], v[4:7], v[100:103], v[48:63]\n\t" \
  "ds_read_b128 v[100:103], v121 offset:62464\n\t" \
  "v_add_f32 v124, v124, v72\n\t" \
  "v_add_f32 v112, v112, v76\n\t" \
  "v_mul_f32 v112, 0x3c000000, v112\n\t" \
  "v_cndmask_b32 v78, v77, 0, s[98:99]\n\t" \
  "v_cndmask_b32 v79, 0, v77, s[98:99]\n\t" \
  "v_fma_f32 v124, -v112, %4, v124\n\t" \
  "s_waitcnt lgkmcnt(3)\n\t" \
  "v_mfma_f32_32x32x16_bf16 v[48:63], v[8:11], v[104:107], v[48:63]\n\t" \
  "ds_read_b128 v[104:107], v121 offset:63488\n\t" \
  "v_cvt_pk_bf16_f32 v64, v32, v33\n\t" \
  "v_cvt_pk_bf16_f32 v65, v34, v35\n\t" \
  "v_cvt_pk_bf16_f32 v66, v36, v37\n\t" \
  "v_cvt_pk_bf16_f32 v67, v38, v39\n\t" \
  "v_mul_f32 v124, v114, v124\n\t" \
  "v_mul_f32 v124, 0x3fb8aa3b, v124\n\t" \
  "s_waitcnt lgkmcnt(3)\n\t" \
  "v_mfma_f32_32x32x16_bf16 v[48:63], v[12:15], v[108:111], v[48:63]\n\t" \
  "ds_read_b128 v[108:111], v121 offset:64512\n\t" \
  "v_cvt_pk_bf16_f32 v68, v40, v41\n\t" \
  "v_cvt_pk_bf16_f32 v69, v42, v43\n\t" \
  "v_cvt_pk_bf16_f32 v70, v44, v45\n\t" \
  "v_cvt_pk_bf16_f32 v71, v46, v47\n\t" \
  "v_cvt_pk_bf16_f32 v116, v116, v116\n\t" \
  "v_cvt_pk_bf16_f32 v117, v117, v117\n\t" \
  "s_waitcnt lgkmcnt(3)\n\t" \
  "v_mfma_f32_32x32x16_bf16 v[48:63], v[16:19], v[96:99], v[48:63]\n\t" \
  "v_cvt_pk_bf16_f32 v118, v118, v118\n\t" \
  "v_cvt_pk_bf16_f32 v119, v119, v119\n\t" \
  "v_lshlrev_b32 v32, 16, v0\n\t" \
  "v_and_b32 v33, 0xffff0000, v0\n\t" \
  "v_lshlrev_b32 v34, 16, v1\n\t" \
  "v_and_b32 v35, 0xffff0000, v1\n\t" \
  "s_waitcnt lgkmcnt(2)\n\t" \
  "v_mfma_f32_32x32x16_bf16 v[48:63], v[20:23], v[100:103], v[48:63]\n\t" \
  "v_lshlrev_b32 v36, 16, v2\n\t" \
  "v_and_b32 v37, 0xffff0000, v2\n\t" \
  "v_lshlrev_b32 v38, 16, v3\n\t" \
  "v_and_b32 v39, 0xffff0000, v3\n\t" \
  "v_lshlrev_b32 v40, 16, v4\n\t" \
  "v_and_b32 v41, 0xffff0000, v4\n\t" \
  "s_waitcnt lgkmcnt(1)\n\t" \
  "v_mfma_f32_32x32x16_bf16 v[48:63], v[24:27], v[104:107], v[48:63]\n\t" \
  "v_lshlrev_b32 v42, 16, v5\n\t" \
  "v_and_b32 v43, 0xffff0000, v5\n\t" \
  "v_lshlrev_b32 v44, 16, v6\n\t" \
  "v_and_b32 v45, 0xffff0000, v6\n\t" \
  "v_lshlrev_b32 v46, 16, v7\n\t" \
  "v_and_b32 v47, 0xffff0000, v7\n\t" \
  "s_waitcnt lgkmcnt(0)\n\t" \
  "v_mfma_f32_32x32x16_bf16 v[48:63], v[28:31], v[108:111], v[48:63]\n\t" \
  "v_and_b32 v96, v116, v78\n\t" \
  "v_and_b32 v97, v116, v79\n\t" \
  "v_and_b32 v98, v117, v78\n\t" \
  "v_and_b32 v99, v117, v79\n\t" \
  "v_and_b32 v100, v118, v78\n\t" \
  "v_and_b32 v101, v118, v79\n\t" \
  "v_and_b32 v102, v119, v78\n\t" \
  "v_and_b32 v103, v119, v79\n\t" \
  "v_mfma_f32_32x32x16_bf16 v[32:47], v[80:83], v[96:99], v[32:47]\n\t" \
  "s_nop 0\n\t" \
  "v_mfma_f32_32x32x16_bf16 v[32:47], v[84:87], v[100:103], v[32:47]\n\t" \
  "s_setprio 2\n\t" \
  "v_cvt_pk_bf16_f32 v72, v48, v49\n\t" \
  "v_cvt_pk_bf16_f32 v73, v50, v51\n\t" \
  "v_cvt_pk_bf16_f32 v74, v52, v53\n\t" \
  "v_cvt_pk_bf16_f32 v75, v54, v55\n\t" \
  "v_cvt_pk_bf16_f32 v76, v56, v57\n\t" \
  "v_cvt_pk_bf16_f32 v77, v58, v59\n\t" \
  "v_cvt_pk_bf16_f32 v78, v60, v61\n\t" \
  "v_cvt_pk_bf16_f32 v79, v62, v63\n\t" \
  "v_lshlrev_b32 v48, 16, v8\n\t" \
  "v_and_b32 v49, 0xffff0000, v8\n\t" \
  "v_lshlrev_b32 v50, 16, v9\n\t" \
  "v_and_b32 v51, 0xffff0000, v9\n\t" \
  "v_lshlrev_b32 v52, 16, v10\n\t" \
  "v_and_b32 v53, 0xffff0000, v10\n\t" \
  "v_lshlrev_b32 v54, 16, v11\n\t" \
  "v_and_b32 v55, 0xffff0000, v11\n\t" \
  "v_lshlrev_b32 v56, 16, v12\n\t" \
  "v_and_b32 v57, 0xffff0000, v12\n\t" \
  "v_lshlrev_b32 v58, 16, v13\n\t" \
  "v_and_b32 v59, 0xffff0000, v13\n\t" \
  "v_lshlrev_b32 v60, 16, v14\n\t" \
  "v_and_b32 v61, 0xffff0000, v14\n\t" \
  "v_lshlrev_b32 v62, 16, v15\n\t" \
  "v_and_b32 v63, 0xffff0000, v15\n\t" \
  "s_nop 1\n\t" \
  "v_mfma_f32_32x32x16_bf16 v[48:63], v[88:91], v[96:99], v[48:63]\n\t" \
  "v_mfma_f32_32x32x16_bf16 v[48:63], v[92:95], v[100:103], v[48:63]\n\t" \
  "v_lshlrev_b32 v0, 16, v16\n\t" \
  "v_and_b32 v1, 0xffff0000, v16\n\t" \
  "v_lshlrev_b32 v2, 16, v17\n\t" \
  "v_and_b32 v3, 0xffff0000, v17\n\t" \
  "v_lshlrev_b32 v4, 16, v18\n\t" \
  "v_and_b32 v5, 0xffff0000, v18\n\t" \
  "v_lshlrev_b32 v6, 16, v19\n\t" \
  "v_and_b32 v7, 0xffff0000, v19\n\t" \
  "v_lshlrev_b32 v8, 16, v20\n\t" \
  "v_and_b32 v9, 0xffff0000, v20\n\t" \
  "v_lshlrev_b32 v10, 16, v21\n\t" \
  "v_and_b32 v11, 0xffff0000, v21\n\t" \
  "v_lshlrev_b32 v12, 16, v22\n\t" \
  "v_and_b32 v13, 0xffff0000, v22\n\t" \
  "v_lshlrev_b32 v14, 16, v23\n\t" \
  "v_and_b32 v15, 0xffff0000, v23\n\t" \
  "s_nop 1\n\t" \
  "v_mfma_f32_32x32x16_bf16 v[0:15], v[64:67], v[96:99], v[0:15]\n\t" \
  "v_mfma_f32_32x32x16_bf16 v[0:15], v[68:71], v[100:103], v[0:15]\n\t" \
  "v_lshlrev_b32 v80, 16, v24\n\t" \
  "v_and_b32 v81, 0xffff0000, v24\n\t" \
  "v_lshlrev_b32 v82, 16, v25\n\t" \
  "v_and_b32 v83, 0xffff0000, v25\n\t" \
  "v_lshlrev_b32 v84, 16, v26\n\t" \
  "v_and_b32 v85, 0xffff0000, v26\n\t" \
  "v_lshlrev_b32 v86, 16, v27\n\t" \
  "v_and_b32 v87, 0xffff0000, v27\n\t" \
  "v_lshlrev_b32 v88, 16, v28\n\t" \
  "v_and_b32 v89, 0xffff0000, v28\n\t" \
  "v_lshlrev_b32 v90, 16, v29\n\t" \
  "v_and_b32 v91, 0xffff0000, v29\n\t" \
  "v_lshlrev_b32 v92, 16, v30\n\t" \
  "v_and_b32 v93, 0xffff0000, v30\n\t" \
  "v_lshlrev_b32 v94, 16, v31\n\t" \
  "v_and_b32 v95, 0xffff0000, v31\n\t" \
  "s_nop 1\n\t" \
  "v_mfma_f32_32x32x16_bf16 v[80:95], v[72:75], v[96:99], v[80:95]\n\t" \
  "v_mfma_f32_32x32x16_bf16 v[80:95], v[76:79], v[100:103], v[80:95]\n\t" \
  "s_setprio 0\n\t" \
  "v_mul_f32 v116, v32, v32\n\t" \
  "v_mul_f32 v117, v33, v33\n\t" \
  "v_fmac_f32 v116, v34, v34\n\t" \
  "v_fmac_f32 v117, v35, v35\n\t" \
  "v_fmac_f32 v116, v36, v36\n\t" \
  "v_fmac_f32 v117, v37, v37\n\t" \
  "v_fmac_f32 v116, v38, v38\n\t" \
  "v_fmac_f32 v117, v39, v39\n\t" \
  "v_fmac_f32 v116, v40, v40\n\t" \
  "v_fmac_f32 v117, v41, v41\n\t" \
  "v_fmac_f32 v116, v42, v42\n\t" \
  "v_fmac_f32 v117, v43, v43\n\t" \
  "v_fmac_f32 v116, v44, v44\n\t" \
  "v_fmac_f32 v117, v45, v45\n\t" \
  "v_fmac_f32 v116, v46, v46\n\t" \
  "v_fmac_f32 v117, v47, v47\n\t" \
  "v_fmac_f32 v116, v48, v48\n\t" \
  "v_fmac_f32 v117, v49, v49\n\t" \
  "v_fmac_f32 v116, v50, v50\n\t" \
  "v_fmac_f32 v117, v51, v51\n\t" \
  "v_fmac_f32 v116, v52, v52\n\t" \
  "v_fmac_f32 v117, v53, v53\n\t" \
  "v_fmac_f32 v116, v54, v54\n\t" \
  "v_fmac_f32 v117, v55, v55\n\t" \
  "v_fmac_f32 v116, v56, v56\n\t" \
  "v_fmac_f32 v117, v57, v57\n\t" \
  "v_fmac_f32 v116, v58, v58\n\t" \
  "v_fmac_f32 v117, v59, v59\n\t" \
  "v_fmac_f32 v116, v60, v60\n\t" \
  "v_fmac_f32 v117, v61, v61\n\t" \
  "v_fmac_f32 v116, v62, v62\n\t" \
  "v_fmac_f32 v117, v63, v63\n\t" \
  "v_fmac_f32 v116, v0, v0\n\t" \
  "v_fmac_f32 v117, v1, v1\n\t" \
  "v_fmac_f32 v116, v2, v2\n\t" \
  "v_fmac_f32 v117, v3, v3\n\t" \
  "v_fmac_f32 v116, v4, v4\n\t" \
  "v_fmac_f32 v117, v5, v5\n\t" \
  "v_fmac_f32 v116, v6, v6\n\t" \
  "v_fmac_f32 v117, v7, v7\n\t" \
  "v_fmac_f32 v116, v8, v8\n\t" \
  "v_fmac_f32 v117, v9, v9\n\t" \
  "v_fmac_f32 v116, v10, v10\n\t" \
  "v_fmac_f32 v117, v11, v11\n\t" \
  "v_fmac_f32 v116, v12, v12\n\t" \
  "v_fmac_f32 v117, v13, v13\n\t" \
  "v_fmac_f32 v116, v14, v14\n\t" \
  "v_fmac_f32 v117, v15, v15\n\t" \
  "v_fmac_f32 v116, v80, v80\n\t" \
  "v_fmac_f32 v117, v81, v81\n\t" \
  "v_fmac_f32 v116, v82, v82\n\t" \
  "v_fmac_f32 v117, v83, v83\n\t" \
  "v_fmac_f32 v116, v84, v84\n\t" \
  "v_fmac_f32 v117, v85, v85\n\t" \
  "v_fmac_f32 v116, v86, v86\n\t" \
  "v_fmac_f32 v117, v87, v87\n\t" \
  "v_fmac_f32 v116, v88, v88\n\t" \
  "v_fmac_f32 v117, v89, v89\n\t" \
  "v_fmac_f32 v116, v90, v90\n\t" \
  "v_fmac_f32 v117, v91, v91\n\t" \
  "v_fmac_f32 v116, v92, v92\n\t" \
  "v_fmac_f32 v117, v93, v93\n\t" \
  "v_fmac_f32 v116, v94, v94\n\t" \
  "v_fmac_f32 v117, v95, v95\n\t" \
  "v_add_f32 v116, v116, v117\n\t" \
  "v_permlane16_swap_b32 v32, v0\n\t" \
  "v_mov_b32 v117, v116\n\t" \
  "v_permlane16_swap_b32 v33, v1\n\t" \
  "v_permlane16_swap_b32 v34, v2\n\t" \
  "v_permlane16_swap_b32 v35, v3\n\t" \
  "v_permlane32_swap_b32 v116, v117\n\t" \
  "v_permlane16_swap_b32 v36, v4\n\t" \
  "v_add_f32 v116, v116, v117\n\t" \
  "v_permlane16_swap_b32 v37, v5\n\t" \
  "v_mul_f32 v116, 0x3c000000, v116\n\t" \
  "v_permlane16_swap_b32 v38, v6\n\t" \
  "v_fma_f32 v116, -v112, v112, v116\n\t" \
  "v_permlane16_swap_b32 v39, v7\n\t" \
  "v_max_f32 v116, 0, v116\n\t" \
  "v_permlane16_swap_b32 v40, v8\n\t" \
  "v_fmaak_f32 v116, v115, v116, 0x3727c5ac\n\t" \
  "v_permlane16_swap_b32 v41, v9\n\t" \
  "v_rsq_f32 v116, v116\n\t" \
  "v_permlane16_swap_b32 v42, v10\n\t" \
  "v_mul_f32 v116, v124, v116\n\t" \
  "v_permlane16_swap_b32 v43, v11\n\t" \
  "v_permlane16_swap_b32 v44, v12\n\t" \
  "v_permlane16_swap_b32 v45, v13\n\t" \
  "v_max_f32_dpp v118, v116, v116 row_ror:8 row_mask:0xf bank_mask:0xf\n\t" \
  "v_permlane16_swap_b32 v46, v14\n\t" \
  "v_permlane16_swap_b32 v47, v15\n\t" \
  "v_permlane16_swap_b32 v48, v80\n\t" \
  "v_max_f32_dpp v118, v118, v118 row_ror:4 row_mask:0xf bank_mask:0xf\n\t" \
  "v_permlane16_swap_b32 v49, v81\n\t" \
  "v_mov_b32 v119, v118\n\t" \
  "v_permlane16_swap_b32 v50, v82\n\t" \
  "v_permlane16_swap_b32 v51, v83\n\t" \
  "v_permlane16_swap_b32 v52, v84\n\t" \
  "v_permlane16_swap_b32 v118, v119\n\t" \
  "v_permlane16_swap_b32 v53, v85\n\t" \
  "v_max_f32 v118, v118, v119\n\t" \
  "v_permlane16_swap_b32 v54, v86\n\t" \
  "v_sub_f32 v116, v116, v118\n\t" \
  "v_permlane16_swap_b32 v55, v87\n\t" \
  "v_exp_f32 v125, v116\n\t" \
  "v_permlane16_swap_b32 v56, v88\n\t" \
  "v_permlane16_swap_b32 v57, v89\n\t" \
  "v_permlane16_swap_b32 v58, v90\n\t" \
  "v_add_f32_dpp v118, v125, v125 row_ror:8 row_mask:0xf bank_mask:0xf\n\t" \
  "v_permlane16_swap_b32 v59, v91\n\t" \
  "v_permlane16_swap_b32 v60, v92\n\t" \
  "v_permlane16_swap_b32 v61, v93\n\t" \
  "v_add_f32_dpp v118, v118, v118 row_ror:4 row_mask:0xf bank_mask:0xf\n\t" \
  "v_permlane16_swap_b32 v62, v94\n\t" \
  "v_mov_b32 v119, v118\n\t" \
  "v_permlane16_swap_b32 v63, v95\n\t" \
  "s_nop 0\n\t" \
  "v_permlane16_swap_b32 v118, v119\n\t" \
  "v_add_f32 v118, v118, v119\n\t" \
  "v_rcp_f32 v118, v118\n\t" \
  "v_mul_f32 v125, v114, v125\n\t" \
  "v_mul_f32 v118, v118, v125\n\t" \
  "v_mov_b32 v119, v118\n\t" \
  "s_nop 1\n\t" \
  "v_permlane16_swap_b32 v118, v119\n\t" \
  "v_mul_f32 v32, v118, v32\n\t" \
  "v_mul_f32 v33, v118, v33\n\t" \
  "v_mul_f32 v34, v118, v34\n\t" \
  "v_mul_f32 v35, v118, v35\n\t" \
  "v_mul_f32 v36, v118, v36\n\t" \
  "v_mul_f32 v37, v118, v37\n\t" \
  "v_mul_f32 v38, v118, v38\n\t" \
  "v_mul_f32 v39, v118, v39\n\t" \
  "v_mul_f32 v40, v118, v40\n\t" \
  "v_mul_f32 v41, v118, v41\n\t" \
  "v_mul_f32 v42, v118, v42\n\t" \
  "v_mul_f32 v43, v118, v43\n\t" \
  "v_mul_f32 v44, v118, v44\n\t" \
  "v_mul_f32 v45, v118, v45\n\t" \
  "v_mul_f32 v46, v118, v46\n\t" \
  "v_mul_f32 v47, v118, v47\n\t" \
  "v_mul_f32 v48, v118, v48\n\t" \
  "v_mul_f32 v49, v118, v49\n\t" \
  "v_mul_f32 v50, v118, v50\n\t" \
  "v_mul_f32 v51, v118, v51\n\t" \
  "v_mul_f32 v52, v118, v52\n\t" \
  "v_mul_f32 v53, v118, v53\n\t" \
  "v_mul_f32 v54, v118, v54\n\t" \
  "v_mul_f32 v55, v118, v55\n\t" \
  "v_mul_f32 v56, v118, v56\n\t" \
  "v_mul_f32 v57, v118, v57\n\t" \
  "v_mul_f32 v58, v118, v58\n\t" \
  "v_mul_f32 v59, v118, v59\n\t" \
  "v_mul_f32 v60, v118, v60\n\t" \
  "v_mul_f32 v61, v118, v61\n\t" \
  "v_mul_f32 v62, v118, v62\n\t" \
  "v_mul_f32 v63, v118, v63\n\t" \
  "v_fmac_f32 v32, v119, v0\n\t" \
  "v_fmac_f32 v33, v119, v1\n\t" \
  "v_fmac_f32 v34, v119, v2\n\t" \
  "v_fmac_f32 v35, v119, v3\n\t" \
  "v_fmac_f32 v36, v119, v4\n\t" \
  "v_fmac_f32 v37, v119, v5\n\t" \
  "v_fmac_f32 v38, v119, v6\n\t" \
  "v_fmac_f32 v39, v119, v7\n\t" \
  "v_fmac_f32 v40, v119, v8\n\t" \
  "v_fmac_f32 v41, v119, v9\n\t" \
  "v_fmac_f32 v42, v119, v10\n\t" \
  "v_fmac_f32 v43, v119, v11\n\t" \
  "v_fmac_f32 v44, v119, v12\n\t" \
  "v_fmac_f32 v45, v119, v13\n\t" \
  "v_fmac_f32 v46, v119, v14\n\t" \
  "v_fmac_f32 v47, v119, v15\n\t" \
  "v_fmac_f32 v48, v119, v80\n\t" \
  "v_fmac_f32 v49, v119, v81\n\t" \
  "v_fmac_f32 v50, v119, v82\n\t" \
  "v_fmac_f32 v51, v119, v83\n\t" \
  "v_fmac_f32 v52, v119, v84\n\t" \
  "v_fmac_f32 v53, v119, v85\n\t" \
  "v_fmac_f32 v54, v119, v86\n\t" \
  "v_fmac_f32 v55, v119, v87\n\t" \
  "v_fmac_f32 v56, v119, v88\n\t" \
  "v_fmac_f32 v57, v119, v89\n\t" \
  "v_fmac_f32 v58, v119, v90\n\t" \
  "v_fmac_f32 v59, v119, v91\n\t" \
  "v_fmac_f32 v60, v119, v92\n\t" \
  "v_fmac_f32 v61, v119, v93\n\t" \
  "v_fmac_f32 v62, v119, v94\n\t" \
  "v_fmac_f32 v63, v119, v95\n\t" \
  "v_add_f32_dpp v32, v32, v32 row_shl:8 row_mask:0xf bank_mask:0x3\n\t" \
  "v_add_f32_dpp v33, v33, v33 row_shl:8 row_mask:0xf bank_mask:0x3\n\t" \
  "v_add_f32_dpp v34, v34, v34 row_shl:8 row_mask:0xf bank_mask:0x3\n\t" \
  "v_add_f32_dpp v35, v35, v35 row_shl:8 row_mask:0xf bank_mask:0x3\n\t" \
  "v_add_f32_dpp v36, v36, v36 row_shl:8 row_mask:0xf bank_mask:0x3\n\t" \
  "v_add_f32_dpp v37, v37, v37 row_shl:8 row_mask:0xf bank_mask:0x3\n\t" \
  "v_add_f32_dpp v38, v38, v38 row_shl:8 row_mask:0xf bank_mask:0x3\n\t" \
  "v_add_f32_dpp v39, v39, v39 row_shl:8 row_mask:0xf bank_mask:0x3\n\t" \
  "v_add_f32_dpp v40, v40, v40 row_shl:8 row_mask:0xf bank_mask:0x3\n\t" \
  "v_add_f32_dpp v41, v41, v41 row_shl:8 row_mask:0xf bank_mask:0x3\n\t" \
  "v_add_f32_dpp v42, v42, v42 row_shl:8 row_mask:0xf bank_mask:0x3\n\t" \
  "v_add_f32_dpp v43, v43, v43 row_shl:8 row_mask:0xf bank_mask:0x3\n\t" \
  "v_add_f32_dpp v44, v44, v44 row_shl:8 row_mask:0xf bank_mask:0x3\n\t" \
  "v_add_f32_dpp v45, v45, v45 row_shl:8 row_mask:0xf bank_mask:0x3\n\t" \
  "v_add_f32_dpp v46, v46, v46 row_shl:8 row_mask:0xf bank_mask:0x3\n\t" \
  "v_add_f32_dpp v47, v47, v47 row_shl:8 row_mask:0xf bank_mask:0x3\n\t" \
  "v_add_f32_dpp v32, v48, v48 row_shr:8 row_mask:0xf bank_mask:0xc\n\t" \
  "v_add_f32_dpp v33, v49, v49 row_shr:8 row_mask:0xf bank_mask:0xc\n\t" \
  "v_add_f32_dpp v34, v50, v50 row_shr:8 row_mask:0xf bank_mask:0xc\n\t" \
  "v_add_f32_dpp v35, v51, v51 row_shr:8 row_mask:0xf bank_mask:0xc\n\t" \
  "v_add_f32_dpp v36, v52, v52 row_shr:8 row_mask:0xf bank_mask:0xc\n\t" \
  "v_add_f32_dpp v37, v53, v53 row_shr:8 row_mask:0xf bank_mask:0xc\n\t" \
  "v_add_f32_dpp v38, v54, v54 row_shr:8 row_mask:0xf bank_mask:0xc\n\t" \
  "v_add_f32_dpp v39, v55, v55 row_shr:8 row_mask:0xf bank_mask:0xc\n\t" \
  "v_add_f32_dpp v40, v56, v56 row_shr:8 row_mask:0xf bank_mask:0xc\n\t" \
  "v_add_f32_dpp v41, v57, v57 row_shr:8 row_mask:0xf bank_mask:0xc\n\t" \
  "v_add_f32_dpp v42, v58, v58 row_shr:8 row_mask:0xf bank_mask:0xc\n\t" \
  "v_add_f32_dpp v43, v59, v59 row_shr:8 row_mask:0xf bank_mask:0xc\n\t" \
  "v_add_f32_dpp v44, v60, v60 row_shr:8 row_mask:0xf bank_mask:0xc\n\t" \
  "v_add_f32_dpp v45, v61, v61 row_shr:8 row_mask:0xf bank_mask:0xc\n\t" \
  "v_add_f32_dpp v46, v62, v62 row_shr:8 row_mask:0xf bank_mask:0xc\n\t" \
  "v_add_f32_dpp v47, v63, v63 row_shr:8 row_mask:0xf bank_mask:0xc\n\t" \
  "v_add_f32_dpp v32, v32, v32 row_shl:4 row_mask:0xf bank_mask:0x5\n\t" \
  "v_add_f32_dpp v33, v33, v33 row_shl:4 row_mask:0xf bank_mask:0x5\n\t" \
  "v_add_f32_dpp v34, v34, v34 row_shl:4 row_mask:0xf bank_mask:0x5\n\t" \
  "v_add_f32_dpp v35, v35, v35 row_shl:4 row_mask:0xf bank_mask:0x5\n\t" \
  "v_add_f32_dpp v36, v36, v36 row_shl:4 row_mask:0xf bank_mask:0x5\n\t" \
  "v_add_f32_dpp v37, v37, v37 row_shl:4 row_mask:0xf bank_mask:0x5\n\t" \
  "v_add_f32_dpp v38, v38, v38 row_shl:4 row_mask:0xf bank_mask:0x5\n\t" \
  "v_add_f32_dpp v39, v39, v39 row_shl:4 row_mask:0xf bank_mask:0x5\n\t" \
  "v_add_f32_dpp v32, v40, v40 row_shr:4 row_mask:0xf bank_mask:0xa\n\t" \
  "v_add_f32_dpp v33, v41, v41 row_shr:4 row_mask:0xf bank_mask:0xa\n\t" \
  "v_add_f32_dpp v34, v42, v42 row_shr:4 row_mask:0xf bank_mask:0xa\n\t" \
  "v_add_f32_dpp v35, v43, v43 row_shr:4 row_mask:0xf bank_mask:0xa\n\t" \
  "v_add_f32_dpp v36, v44, v44 row_shr:4 row_mask:0xf bank_mask:0xa\n\t" \
  "v_add_f32_dpp v37, v45, v45 row_shr:4 row_mask:0xf bank_mask:0xa\n\t" \
  "v_add_f32_dpp v38, v46, v46 row_shr:4 row_mask:0xf bank_mask:0xa\n\t" \
  "v_add_f32_dpp v39, v47, v47 row_shr:4 row_mask:0xf bank_mask:0xa\n\t" \
  "v_cvt_pk_bf16_f32 v96, v32, v33\n\t" \
  "v_cvt_pk_bf16_f32 v97, v34, v35\n\t" \
  "v_cvt_pk_bf16_f32 v98, v36, v37\n\t" \
  "v_cvt_pk_bf16_f32 v99, v38, v39\n\t" \
  "s_branch .Lend_%=\n\t" \
  ".Lh_%=:\n\t" \
  "s_setprio 3\n\t" \
  "global_load_ushort v120, v117, %8\n\t" \
  "s_mov_b64 exec, s[94:95]\n\t" \
  "buffer_load_dwordx4 v[0:3], v112, %7, 0 offen\n\t" \
  "buffer_load_dwordx4 v[4:7], v112, %7, 0 offen offset:32\n\t" \
  "s_mov_b64 exec, -1\n\t" \
  "v_mov_b32 v115, 153520\n\t" \
  "v_mov_b32 v119, 1\n\t" \
  ".Lqa_%=:\n\t" \
  "ds_read_b32 v125, v115 offset:0\n\t" \
  "s_waitcnt lgkmcnt(0)\n\t" \
  "v_cmp_gt_u32 vcc, 5, v125\n\t" \
  "s_cbranch_vccz .Lqad_%=\n\t" \
  "s_sleep 1\n\t" \
  "s_branch .Lqa_%=\n\t" \
  ".Lqad_%=:\n\t" \
  "s_not_b64 exec, s[94:95]\n\t" \
  "s_mov_b32 %4, 0\n\t" \
  ".Lqr_%=:\n\t" \
  "s_mov_b32 %2, 0\n\t" \
  "buffer_load_dwordx2 v[0:1], v114, %6, %2 offen sc1\n\t" \
  "s_mov_b32 %2, 100480\n\t" \
  "buffer_load_dwordx2 v[2:3], v114, %6, %2 offen sc1\n\t" \
  "s_mov_b32 %2, 401920\n\t" \
  "buffer_load_dwordx2 v[4:5], v114, %6, %2 offen sc1\n\t" \
  "s_mov_b32 %2, 502400\n\t" \
  "buffer_load_dwordx2 v[6:7], v114, %6, %2 offen sc1\n\t" \
  "s_waitcnt vmcnt(0)\n\t" \
  "v_max3_u32 v125, v0, v1, 0\n\t" \
  "v_max3_u32 v125, v2, v3, v125\n\t" \
  "v_max3_u32 v125, v4, v5, v125\n\t" \
  "v_max3_u32 v125, v6, v7, v125\n\t" \
  "v_cmp_eq_u32 vcc, -1, v125\n\t" \
  "s_cbranch_vccz .Lqk_%=\n\t" \
  "s_sleep 8\n\t" \
  "s_add_u32 %4, %4, 1\n\t" \
  "s_cmp_gt_u32 %4, 0x80000\n\t" \
  "s_cbranch_scc0 .Lqr_%=\n\t" \
  ".Lqk_%=:\n\t" \
  "s_mov_b64 exec, -1\n\t" \
  "ds_read_b128 v[40:43], v118\n\t" \
  "ds_read_b128 v[44:47], v118 offset:32\n\t" \
  "s_sub_u32 %2, %5, 12\n\t" \
  "s_lshl_b32 %2, %2, 11\n\t" \
  "v_add_u32 v113, 65536, v121\n\t" \
  "v_add_u32 v116, %2, v113\n\t" \
  "v_lshl_add_u32 v114, %2, 2, v121\n\t" \
  "v_mfma_f32_4x4x4_16b_bf16 v[48:51], v[0:1], v[0:1], 0\n\t" \
  "v_mfma_f32_4x4x4_16b_bf16 v[52:55], v[2:3], v[2:3], 0\n\t" \
  "s_nop 0\n\t" \
  "v_mfma_f32_4x4x4_16b_bf16 v[48:51], v[4:5], v[4:5], v[48:51]\n\t" \
  "v_mfma_f32_4x4x4_16b_bf16 v[52:55], v[6:7], v[6:7], v[52:55]\n\t" \
  "s_waitcnt lgkmcnt(1)\n\t" \
  "v_mfma_f32_16x16x32_bf16 v[56:59], v[40:43], v[0:3], 0\n\t" \
  "s_waitcnt lgkmcnt(0)\n\t" \
  "v_mfma_f32_16x16x32_bf16 v[56:59], v[44:47], v[4:7], v[56:59]\n\t" \
  ".Lqb_%=:\n\t" \
  "ds_read_b32 v125, v115 offset:20\n\t" \
  "s_waitcnt lgkmcnt(0)\n\t" \
  "v_cmp_gt_u32 vcc, 4, v125\n\t" \
  "s_cbranch_vccz .Lqbd_%=\n\t" \
  "s_sleep 1\n\t" \
  "s_branch .Lqb_%=\n\t" \
  ".Lqbd_%=:\n\t" \
  "ds_write_b128 v116, v[0:3]\n\t" \
  "ds_write_b128 v116, v[4:7] offset:1024\n\t" \
  "s_lshr_b32 %2, %2, 1\n\t" \
  "v_add_u32 v116, %2, v113\n\t" \
  "s_lshr_b32 %2, %2, 8\n\t" \
  "v_add_u32 v117, %2, v113\n\t" \
  "v_add_f32 v48, v48, v52\n\t" \
  "v_add_f32 v49, v49, v53\n\t" \
  "v_add_f32 v50, v50, v54\n\t" \
  "v_add_f32 v51, v51, v55\n\t" \
  "v_cndmask_b32 v48, v48, v49, s[96:97]\n\t" \
  "v_cndmask_b32 v50, v50, v51, s[96:97]\n\t" \
  "v_cndmask_b32 v48, v48, v50, s[98:99]\n\t" \
  "ds_write_b128 v116, v[56:59] offset:12288\n\t" \
  "ds_write_b32 v117, v48 offset:16384\n\t" \
  "v_lshlrev_b32 v120, 16, v120\n\t" \
  "v_cmp_eq_f32 vcc, 0, v120\n\t" \
  "s_or_b64 %3, vcc, s[94:95]\n\t" \
  "v_cndmask_b32 v120, v120, 1.0, %3\n\t" \
  ".Lqc_%=:\n\t" \
  "ds_read_b32 v125, v115 offset:12\n\t" \
  "s_waitcnt lgkmcnt(0)\n\t" \
  "v_cmp_gt_u32 vcc, 4, v125\n\t" \
  "s_cbranch_vccz .Lqcd_%=\n\t" \
  "s_sleep 1\n\t" \
  "s_branch .Lqc_%=\n\t" \
  ".Lqcd_%=:\n\t" \
  "s_waitcnt lgkmcnt(0)\n\t" \
  "s_mov_b64 exec, 1\n\t" \
  "ds_add_u32 v115, v119 offset:24\n\t" \
  "s_mov_b64 exec, -1\n\t" \
  ".Lqd_%=:\n\t" \
  "ds_read_b32 v125, v115 offset:24\n\t" \
  "s_waitcnt lgkmcnt(0)\n\t" \
  "v_cmp_gt_u32 vcc, 4, v125\n\t" \
  "s_cbranch_vccz .Lqdd_%=\n\t" \
  "s_sleep 1\n\t" \
  "s_branch .Lqd_%=\n\t" \
  ".Lqdd_%=:\n\t" \
  "ds_read_b128 v[8:11], v113 offset:0\n\t" \
  "ds_read_b128 v[40:43], v114 offset:0\n\t" \
  "ds_read_b128 v[12:15], v113 offset:1024\n\t" \
  "ds_read_b128 v[44:47], v114 offset:1024\n\t" \
  "ds_read_b128 v[16:19], v113 offset:2048\n\t" \
  "ds_read_b128 v[48:51], v114 offset:2048\n\t" \
  "ds_read_b128 v[20:23], v113 offset:3072\n\t" \
  "ds_read_b128 v[52:55], v114 offset:3072\n\t" \
  "ds_read_b128 v[24:27], v113 offset:4096\n\t" \
  "ds_read_b128 v[56:59], v114 offset:4096\n\t" \
  "ds_read_b128 v[28:31], v113 offset:5120\n\t" \
  "ds_read_b128 v[60:63], v114 offset:5120\n\t" \
  "s_waitcnt lgkmcnt(10)\n\t" \
  "v_mfma_f32_32x32x16_bf16 v[72:87], v[40:43], v[8:11], 0\n\t" \
  "s_waitcnt lgkmcnt(8)\n\t" \
  "v_mfma_f32_32x32x16_bf16 v[72:87], v[44:47], v[12:15], v[72:87]\n\t" \
  "ds_read_b128 v[32:35], v113 offset:6144\n\t" \
  "ds_read_b128 v[64:67], v114 offset:6144\n\t" \
  "ds_read_b128 v[36:39], v113 offset:7168\n\t" \
  "ds_read_b128 v[68:71], v114 offset:7168\n\t" \
  "ds_read_b128 v[40:43], v114 offset:32768\n\t" \
  "ds_read_b128 v[44:47], v114 offset:33792\n\t" \
  "s_waitcnt lgkmcnt(12)\n\t" \
  "v_mfma_f32_32x32x16_bf16 v[72:87], v[48:51], v[16:19], v[72:87]\n\t" \
  "ds_read_b128 v[48:51], v114 offset:34816\n\t" \
  "s_waitcnt lgkmcnt(11)\n\t" \
  "v_mfma_f32_32x32x16_bf16 v[72:87], v[52:55], v[20:23], v[72:87]\n\t" \
  "ds_read_b128 v[52:55], v114 offset:35840\n\t" \
  "s_waitcnt lgkmcnt(10)\n\t" \
  "v_mfma_f32_32x32x16_bf16 v[72:87], v[56:59], v[24:27], v[72:87]\n\t" \
  "ds_read_b128 v[56:59], v114 offset:36864\n\t" \
  "s_waitcnt lgkmcnt(9)\n\t" \
  "v_mfma_f32_32x32x16_bf16 v[72:87], v[60:63], v[28:31], v[72:87]\n\t" \
  "ds_read_b128 v[60:63], v114 offset:37888\n\t" \
  "s_waitcnt lgkmcnt(8)\n\t" \
  "v_mfma_f32_32x32x16_bf16 v[72:87], v[64:67], v[32:35], v[72:87]\n\t" \
  "ds_read_b128 v[64:67], v114 offset:38912\n\t" \
  "s_waitcnt lgkmcnt(7)\n\t" \
  "v_mfma_f32_32x32x16_bf16 v[72:87], v[68:71], v[36:39], v[72:87]\n\t" \
  "ds_read_b128 v[68:71], v114 offset:39936\n\t" \
  "s_waitcnt lgkmcnt(7)\n\t" \
  "v_mfma_f32_32x32x16_bf16 v[88:103], v[8:11], v[40:43], 0\n\t" \
  "s_waitcnt lgkmcnt(6)\n\t" \
  "v_mfma_f32_32x32x16_bf16 v[88:103], v[12:15], v[44:47], v[88:103]\n\t" \
  "s_waitcnt lgkmcnt(5)\n\t" \
  "v_mfma_f32_32x32x16_bf16 v[88:103], v[16:19], v[48:51], v[88:103]\n\t" \
  "s_nop 4\n\t" \
  "v_cvt_pk_bf16_f32 v104, v72, v73\n\t" \
  "v_cvt_pk_bf16_f32 v105, v74, v75\n\t" \
  "v_cvt_pk_bf16_f32 v106, v76, v77\n\t" \
  "v_cvt_pk_bf16_f32 v107, v78, v79\n\t" \
  "v_cvt_pk_bf16_f32 v108, v80, v81\n\t" \
  "v_cvt_pk_bf16_f32 v109, v82, v83\n\t" \
  "v_cvt_pk_bf16_f32 v110, v84, v85\n\t" \
  "v_cvt_pk_bf16_f32 v111, v86, v87\n\t" \
  "s_waitcnt lgkmcnt(4)\n\t" \
  "v_mfma_f32_32x32x16_bf16 v[88:103], v[20:23], v[52:55], v[88:103]\n\t" \
  "v_mfma_f32_32x32x16_bf16 v[72:87], v[0:3], v[104:107], 0\n\t" \
  "v_mfma_f32_32x32x16_bf16 v[72:87], v[4:7], v[108:111], v[72:87]\n\t" \
  "s_waitcnt lgkmcnt(3)\n\t" \
  "v_mfma_f32_32x32x16_bf16 v[88:103], v[24:27], v[56:59], v[88:103]\n\t" \
  "s_waitcnt lgkmcnt(2)\n\t" \
  "v_mfma_f32_32x32x16_bf16 v[88:103], v[28:31], v[60:63], v[88:103]\n\t" \
  "s_waitcnt lgkmcnt(1)\n\t" \
  "v_mfma_f32_32x32x16_bf16 v[88:103], v[32:35], v[64:67], v[88:103]\n\t" \
  "s_waitcnt lgkmcnt(0)\n\t" \
  "v_mfma_f32_32x32x16_bf16 v[88:103], v[36:39], v[68:71], v[88:103]\n\t" \
  "s_setprio 1\n\t" \
  "ds_read_b128 v[40:43], v113 offset:12288\n\t" \
  "ds_read_b128 v[44:47], v113 offset:13312\n\t" \
  "ds_read_b128 v[48:51], v113 offset:14336\n\t" \
  "ds_read_b128 v[52:55], v113 offset:15360\n\t" \
  "ds_read_b128 v[56:59], v113 offset:16384\n\t" \
  "v_lshlrev_b32 v8, 16, v0\n\t" \
  "v_and_b32 v9, 0xffff0000, v0\n\t" \
  "v_lshlrev_b32 v10, 16, v1\n\t" \
  "v_and_b32 v11, 0xffff0000, v1\n\t" \
  "v_lshlrev_b32 v12, 16, v2\n\t" \
  "v_and_b32 v13, 0xffff0000, v2\n\t" \
  "v_lshlrev_b32 v14, 16, v3\n\t" \
  "v_and_b32 v15, 0xffff0000, v3\n\t" \
  "v_lshlrev_b32 v16, 16, v4\n\t" \
  "v_and_b32 v17, 0xffff0000, v4\n\t" \
  "v_lshlrev_b32 v18, 16, v5\n\t" \
  "v_and_b32 v19, 0xffff0000, v5\n\t" \
  "v_lshlrev_b32 v20, 16, v6\n\t" \
  "v_and_b32 v21, 0xffff0000, v6\n\t" \
  "v_lshlrev_b32 v22, 16, v7\n\t" \
  "v_and_b32 v23, 0xffff0000, v7\n\t" \
  "v_cndmask_b32 v72, v72, v73, s[96:97]\n\t" \
  "v_cndmask_b32 v74, v74, v75, s[96:97]\n\t" \
  "v_cndmask_b32 v76, v76, v77, s[96:97]\n\t" \
  "v_cndmask_b32 v78, v78, v79, s[96:97]\n\t" \
  "v_cndmask_b32 v80, v80, v81, s[96:97]\n\t" \
  "v_cndmask_b32 v82, v82, v83, s[96:97]\n\t" \
  "v_cndmask_b32 v84, v84, v85, s[96:97]\n\t" \
  "v_cndmask_b32 v86, v86, v87, s[96:97]\n\t" \
  "v_cndmask_b32 v104, v72, v74, s[98:99]\n\t" \
  "v_cndmask_b32 v105, v76, v78, s[98:99]\n\t" \
  "v_cndmask_b32 v106, v80, v82, s[98:99]\n\t" \
  "v_cndmask_b32 v107, v84, v86, s[98:99]\n\t" \
  "ds_write_b128 v116, v[104:107] offset:8192\n\t" \
  "v_mov_b32 v60, v113\n\t" \
  "v_mov_b32 v61, v115\n\t" \
  "v_mov_b32 v62, v117\n\t" \
  "v_mov_b32 v63, 1\n\t" \
  "s_waitcnt lgkmcnt(0)\n\t" \
  "s_mov_b64 exec, 1\n\t" \
  "ds_add_u32 v61, v63 offset:24\n\t" \
  "s_mov_b64 exec, -1\n\t" \
  "v_add_f32 v40, v40, v44\n\t" \
  "v_add_f32 v48, v48, v52\n\t" \
  "v_add_f32 v41, v41, v45\n\t" \
  "v_add_f32 v49, v49, v53\n\t" \
  "v_add_f32 v42, v42, v46\n\t" \
  "v_add_f32 v50, v50, v54\n\t" \
  "v_add_f32 v43, v43, v47\n\t" \
  "v_add_f32 v51, v51, v55\n\t" \
  "v_add_f32 v112, v40, v48\n\t" \
  "v_add_f32 v113, v41, v49\n\t" \
  "v_add_f32 v114, v42, v50\n\t" \
  "v_add_f32 v115, v43, v51\n\t" \
  "v_add_f32 v56, v56, v57\n\t" \
  "v_add_f32 v58, v58, v59\n\t" \
  "v_add_f32 v124, v56, v58\n\t" \
  "v_rcp_f32 v42, v120\n\t" \
  "v_mov_b32 v40, v124\n\t" \
  "v_mul_f32 v43, v42, v42\n\t" \
  "v_mul_f32 v41, 0x3c000000, v112\n\t" \
  "v_permlane32_swap_b32 v124, v40\n\t" \
  "v_add_f32 v40, v124, v40\n\t" \
  "v_mul_f32 v40, 0x3c000000, v40\n\t" \
  "v_fma_f32 v40, -v41, v41, v40\n\t" \
  "v_max_f32 v40, 0, v40\n\t" \
  "v_fmaak_f32 v40, v43, v40, 0x3727c5ac\n\t" \
  "v_rsq_f32 v40, v40\n\t" \
  "v_mov_b32 v124, v113\n\t" \
  "v_mul_f32 v113, v42, v40\n\t" \
  "ds_write_b128 v122, v[112:115]\n\t" \
  "v_cvt_pk_bf16_f32 v24, v88, v89\n\t" \
  "v_cvt_pk_bf16_f32 v25, v90, v91\n\t" \
  "v_cvt_pk_bf16_f32 v26, v92, v93\n\t" \
  "v_cvt_pk_bf16_f32 v27, v94, v95\n\t" \
  "v_cvt_pk_bf16_f32 v28, v96, v97\n\t" \
  "v_cvt_pk_bf16_f32 v29, v98, v99\n\t" \
  "v_cvt_pk_bf16_f32 v30, v100, v101\n\t" \
  "v_cvt_pk_bf16_f32 v31, v102, v103\n\t" \
  "v_mul_f32 v113, 0x3e0293ee, v113\n\t" \
  "v_mov_b32 v114, v42\n\t" \
  "v_mov_b32 v115, v43\n\t" \
  "ds_read_b128 v[64:67], v123 offset:0\n\t" \
  "ds_read_b128 v[68:71], v123 offset:128\n\t" \
  "ds_read_b128 v[72:75], v123 offset:256\n\t" \
  "ds_read_b128 v[76:79], v123 offset:384\n\t" \
  "v_mov_b32 v44, 153504\n\t" \
  "ds_read_b32 v44, v44\n\t" \
  ".Lqe_%=:\n\t" \
  "ds_read_b32 v125, v61 offset:24\n\t" \
  "s_waitcnt lgkmcnt(0)\n\t" \
  "v_cmp_gt_u32 vcc, 8, v125\n\t" \
  "s_cbranch_vccz .Lqed_%=\n\t" \
  "s_sleep 1\n\t" \
  "s_branch .Lqe_%=\n\t" \
  ".Lqed_%=:\n\t" \
  "v_readfirstlane_b32 %4, v44\n\t" \
  "ds_read_b128 v[40:43], v60 offset:8192\n\t" \
  "ds_read_b128 v[44:47], v60 offset:9216\n\t" \
  "ds_read_b128 v[48:51], v60 offset:10240\n\t" \
  "ds_read_b128 v[52:55], v60 offset:11264\n\t" \
  "s_waitcnt lgkmcnt(2)\n\t" \
  "v_add_f32 v40, v40, v44\n\t" \
  "s_waitcnt lgkmcnt(0)\n\t" \
  "v_add_f32 v48, v48, v52\n\t" \
  "v_add_f32 v41, v41, v45\n\t" \
  "v_add_f32 v49, v49, v53\n\t" \
  "v_add_f32 v42, v42, v46\n\t" \
  "v_add_f32 v50, v50, v54\n\t" \
  "v_add_f32 v43, v43, v47\n\t" \
  "v_add_f32 v51, v51, v55\n\t" \
  "v_add_f32 v116, v40, v48\n\t" \
  "v_add_f32 v117, v41, v49\n\t" \
  "v_add_f32 v118, v42, v50\n\t" \
  "v_add_f32 v119, v43, v51\n\t" \
  "v_mul_f32 v116, v113, v116\n\t" \
  "v_mul_f32 v117, v113, v117\n\t" \
  "v_mul_f32 v118, v113, v118\n\t" \
  "v_mul_f32 v119, v113, v119\n\t" \
  "v_mul_f32 v116, v65, v116\n\t" \
  "v_mul_f32 v117, v69, v117\n\t" \
  "v_mul_f32 v118, v73, v118\n\t" \
  "v_mul_f32 v119, v77, v119\n\t" \
  "v_max3_f32 v64, v116, v117, v118\n\t" \
  "v_max_f32 v64, v64, v119\n\t" \
  "v_mov_b32 v68, v64\n\t" \
  "s_nop 1\n\t" \
  "v_permlane32_swap_b32 v64, v68\n\t" \
  "v_max_f32 v64, v64, v68\n\t" \
  "v_sub_f32 v116, v116, v64\n\t" \
  "v_sub_f32 v117, v117, v64\n\t" \
  "v_sub_f32 v118, v118, v64\n\t" \
  "v_sub_f32 v119, v119, v64\n\t" \
  "v_exp_f32 v116, v116\n\t" \
  "v_exp_f32 v117, v117\n\t" \
  "v_exp_f32 v118, v118\n\t" \
  "v_exp_f32 v119, v119\n\t" \
  "v_add_f32 v64, v116, v117\n\t" \
  "v_add_f32 v68, v118, v119\n\t" \
  "v_add_f32 v64, v64, v68\n\t" \
  "v_mov_b32 v68, v64\n\t" \
  "v_mul_f32 v116, v65, v116\n\t" \
  "v_mul_f32 v117, v69, v117\n\t" \
  "v_mul_f32 v118, v73, v118\n\t" \
  "v_mul_f32 v119, v77, v119\n\t" \
  "v_permlane32_swap_b32 v64, v68\n\t" \
  "v_add_f32 v64, v64, v68\n\t" \
  "v_rcp_f32 v64, v64\n\t" \
  "v_mul_f32 v72, v116, v66\n\t" \
  "v_mul_f32 v64, v120, v64\n\t" \
  "v_mul_f32 v76, v116, v67\n\t" \
  "v_fmac_f32 v72, v117, v70\n\t" \
  "v_fmac_f32 v76, v117, v71\n\t" \
  "v_fmac_f32 v72, v118, v74\n\t" \
  "v_fmac_f32 v76, v118, v75\n\t" \
  "v_fmac_f32 v72, v119, v78\n\t" \
  "v_fmac_f32 v76, v119, v79\n\t" \
  "v_mul_f32 v116, v64, v116\n\t" \
  "v_mul_f32 v117, v64, v117\n\t" \
  "v_mul_f32 v118, v64, v118\n\t" \
  "v_mul_f32 v119, v64, v119\n\t" \
  "v_permlane32_swap_b32 v72, v76\n\t" \
  "v_add_f32 v72, v72, v76\n\t" \
  "v_mul_f32 v72, v64, v72\n\t" \
  "v_mov_b32 v77, 0xffff\n\t" \
  "v_mov_b32 v76, v72\n\t" \
  "v_not_b32 v78, v77\n\t" \
  "v_cndmask_b32 v77, v77, v78, s[96:97]\n\t" \
  "v_permlane32_swap_b32 v72, v76\n\t" \
  "v_add_f32 v124, v124, v72\n\t" \
  "v_add_f32 v112, v112, v76\n\t" \
  "v_mul_f32 v112, 0x3c000000, v112\n\t" \
  "v_cndmask_b32 v78, v77, 0, s[98:99]\n\t" \
  "v_cndmask_b32 v79, 0, v77, s[98:99]\n\t" \
  "v_fma_f32 v124, -v112, %4, v124\n\t" \
  "v_cvt_pk_bf16_f32 v116, v116, v116\n\t" \
  "v_cvt_pk_bf16_f32 v117, v117, v117\n\t" \
  "v_cvt_pk_bf16_f32 v118, v118, v118\n\t" \
  "v_cvt_pk_bf16_f32 v119, v119, v119\n\t" \
  "v_and_b32 v32, v116, v78\n\t" \
  "v_and_b32 v33, v116, v79\n\t" \
  "v_and_b32 v34, v117, v78\n\t" \
  "v_and_b32 v35, v117, v79\n\t" \
  "v_and_b32 v36, v118, v78\n\t" \
  "v_and_b32 v37, v118, v79\n\t" \
  "v_and_b32 v38, v119, v78\n\t" \
  "v_and_b32 v39, v119, v79\n\t" \
  "v_mfma_f32_32x32x16_bf16 v[8:23], v[24:27], v[32:35], v[8:23]\n\t" \
  "s_nop 0\n\t" \
  "v_mfma_f32_32x32x16_bf16 v[8:23], v[28:31], v[36:39], v[8:23]\n\t" \
  "v_mul_f32 v124, v114, v124\n\t" \
  "v_mul_f32 v124, 0x3fb8aa3b, v124\n\t" \
  "s_nop 9\n\t" \
  "v_mul_f32 v116, v8, v8\n\t" \
  "v_mul_f32 v117, v9, v9\n\t" \
  "v_fmac_f32 v116, v10, v10\n\t" \
  "v_fmac_f32 v117, v11, v11\n\t" \
  "v_fmac_f32 v116, v12, v12\n\t" \
  "v_fmac_f32 v117, v13, v13\n\t" \
  "v_fmac_f32 v116, v14, v14\n\t" \
  "v_fmac_f32 v117, v15, v15\n\t" \
  "v_fmac_f32 v116, v16, v16\n\t" \
  "v_fmac_f32 v117, v17, v17\n\t" \
  "v_fmac_f32 v116, v18, v18\n\t" \
  "v_fmac_f32 v117, v19, v19\n\t" \
  "v_fmac_f32 v116, v20, v20\n\t" \
  "v_fmac_f32 v117, v21, v21\n\t" \
  "v_fmac_f32 v116, v22, v22\n\t" \
  "v_fmac_f32 v117, v23, v23\n\t" \
  "v_add_f32 v116, v116, v117\n\t" \
  "ds_write_b32 v62, v116 offset:17408\n\t" \
  "v_permlane16_swap_b32 v8, v16\n\t" \
  "v_permlane16_swap_b32 v9, v17\n\t" \
  "v_permlane16_swap_b32 v10, v18\n\t" \
  "v_permlane16_swap_b32 v11, v19\n\t" \
  "v_permlane16_swap_b32 v12, v20\n\t" \
  "v_permlane16_swap_b32 v13, v21\n\t" \
  "v_permlane16_swap_b32 v14, v22\n\t" \
  "v_permlane16_swap_b32 v15, v23\n\t" \
  "s_waitcnt lgkmcnt(0)\n\t" \
  "s_mov_b64 exec, 1\n\t" \
  "ds_add_u32 v61, v63 offset:24\n\t" \
  "s_mov_b64 exec, -1\n\t" \
  ".Lqf_%=:\n\t" \
  "ds_read_b32 v125, v61 offset:24\n\t" \
  "s_waitcnt lgkmcnt(0)\n\t" \
  "v_cmp_gt_u32 vcc, 12, v125\n\t" \
  "s_cbranch_vccz .Lqfd_%=\n\t" \
  "s_sleep 1\n\t" \
  "s_branch .Lqf_%=\n\t" \
  ".Lqfd_%=:\n\t" \
  "ds_read_b128 v[40:43], v60 offset:17408\n\t" \
  "s_waitcnt lgkmcnt(0)\n\t" \
  "v_add_f32 v116, v40, v41\n\t" \
  "v_add_f32 v117, v42, v43\n\t" \
  "v_add_f32 v116, v116, v117\n\t" \
  "v_mov_b32 v117, v116\n\t" \
  "s_nop 1\n\t" \
  "v_permlane32_swap_b32 v116, v117\n\t" \
  "v_add_f32 v116, v116, v117\n\t" \
  "v_mul_f32 v116, 0x3c000000, v116\n\t" \
  "v_fma_f32 v116, -v112, v112, v116\n\t" \
  "v_max_f32 v116, 0, v116\n\t" \
  "v_fmaak_f32 v116, v115, v116, 0x3727c5ac\n\t" \
  "v_rsq_f32 v116, v116\n\t" \
  "s_nop 0\n\t" \
  "v_mul_f32 v116, v124, v116\n\t" \
  "s_nop 1\n\t" \
  "v_max_f32_dpp v118, v116, v116 row_ror:8 row_mask:0xf bank_mask:0xf\n\t" \
  "s_nop 1\n\t" \
  "v_max_f32_dpp v118, v118, v118 row_ror:4 row_mask:0xf bank_mask:0xf\n\t" \
  "v_mov_b32 v119, v118\n\t" \
  "s_nop 1\n\t" \
  "v_permlane16_swap_b32 v118, v119\n\t" \
  "v_max_f32 v118, v118, v119\n\t" \
  "v_sub_f32 v116, v116, v118\n\t" \
  "v_exp_f32 v125, v116\n\t" \
  "s_nop 1\n\t" \
  "v_add_f32_dpp v118, v125, v125 row_ror:8 row_mask:0xf bank_mask:0xf\n\t" \
  "s_nop 1\n\t" \
  "v_add_f32_dpp v118, v118, v118 row_ror:4 row_mask:0xf bank_mask:0xf\n\t" \
  "v_mov_b32 v119, v118\n\t" \
  "s_nop 1\n\t" \
  "v_permlane16_swap_b32 v118, v119\n\t" \
  "v_add_f32 v118, v118, v119\n\t" \
  "v_rcp_f32 v118, v118\n\t" \
  "v_mul_f32 v125, v114, v125\n\t" \
  "v_mul_f32 v118, v118, v125\n\t" \
  "v_mov_b32 v119, v118\n\t" \
  "s_nop 1\n\t" \
  "v_permlane16_swap_b32 v118, v119\n\t" \
  "v_mul_f32 v8, v118, v8\n\t" \
  "v_mul_f32 v9, v118, v9\n\t" \
  "v_mul_f32 v10, v118, v10\n\t" \
  "v_mul_f32 v11, v118, v11\n\t" \
  "v_mul_f32 v12, v118, v12\n\t" \
  "v_mul_f32 v13, v118, v13\n\t" \
  "v_mul_f32 v14, v118, v14\n\t" \
  "v_mul_f32 v15, v118, v15\n\t" \
  "v_fmac_f32 v8, v119, v16\n\t" \
  "v_fmac_f32 v9, v119, v17\n\t" \
  "v_fmac_f32 v10, v119, v18\n\t" \
  "v_fmac_f32 v11, v119, v19\n\t" \
  "v_fmac_f32 v12, v119, v20\n\t" \
  "v_fmac_f32 v13, v119, v21\n\t" \
  "v_fmac_f32 v14, v119, v22\n\t" \
  "v_fmac_f32 v15, v119, v23\n\t" \
  "v_add_f32_dpp v8, v8, v8 row_ror:8 row_mask:0xf bank_mask:0xf\n\t" \
  "v_add_f32_dpp v9, v9, v9 row_ror:8 row_mask:0xf bank_mask:0xf\n\t" \
  "v_add_f32_dpp v10, v10, v10 row_ror:8 row_mask:0xf bank_mask:0xf\n\t" \
  "v_add_f32_dpp v11, v11, v11 row_ror:8 row_mask:0xf bank_mask:0xf\n\t" \
  "v_add_f32_dpp v12, v12, v12 row_ror:8 row_mask:0xf bank_mask:0xf\n\t" \
  "v_add_f32_dpp v13, v13, v13 row_ror:8 row_mask:0xf bank_mask:0xf\n\t" \
  "v_add_f32_dpp v14, v14, v14 row_ror:8 row_mask:0xf bank_mask:0xf\n\t" \
  "v_add_f32_dpp v15, v15, v15 row_ror:8 row_mask:0xf bank_mask:0xf\n\t" \
  "v_add_f32_dpp v8, v8, v8 row_ror:4 row_mask:0xf bank_mask:0xf\n\t" \
  "v_add_f32_dpp v9, v9, v9 row_ror:4 row_mask:0xf bank_mask:0xf\n\t" \
  "v_add_f32_dpp v10, v10, v10 row_ror:4 row_mask:0xf bank_mask:0xf\n\t" \
  "v_add_f32_dpp v11, v11, v11 row_ror:4 row_mask:0xf bank_mask:0xf\n\t" \
  "v_add_f32_dpp v12, v12, v12 row_ror:4 row_mask:0xf bank_mask:0xf\n\t" \
  "v_add_f32_dpp v13, v13, v13 row_ror:4 row_mask:0xf bank_mask:0xf\n\t" \
  "v_add_f32_dpp v14, v14, v14 row_ror:4 row_mask:0xf bank_mask:0xf\n\t" \
  "v_add_f32_dpp v15, v15, v15 row_ror:4 row_mask:0xf bank_mask:0xf\n\t" \
  "v_cvt_pk_bf16_f32 v96, v8, v9\n\t" \
  "v_cvt_pk_bf16_f32 v97, v10, v11\n\t" \
  "v_cvt_pk_bf16_f32 v98, v12, v13\n\t" \
  "v_cvt_pk_bf16_f32 v99, v14, v15\n\t" \
  ".Lend_%=:\n\t" \
  : "={v[96:99]}"(OUT), "+{v[112:123]}"(C12), "=&s"(T1), "=&s"(T2), "=&s"(SWR) \
  : "s"(W), "s"(RSRC), "s"(RSX), "s"(DIS), "s"(TM0), "s"(TSO), "s"(WSO), "s"(FO), "s"(FL), "s"(T7), "s"(ACT), "s"(FP) \
  : "memory", "scc", "vcc", "s90", "s91", "s94", "s95", "s96", "s97", "s98", "s99", "v0", "v1", "v2", "v3", "v4", "v5", "v6", "v7", "v8", "v9", "v10", "v11", "v12", "v13", "v14", "v15", "v16", "v17", "v18", "v19", "v20", "v21", "v22", "v23", "v24", "v25", "v26", "v27", "v28", "v29", "v30", "v31", "v32", "v33", "v34", "v35", "v36", "v37", "v38", "v39", "v40", "v41", "v42", "v43", "v44", "v45", "v46", "v47", "v48", "v49", "v50", "v51", "v52", "v53", "v54", "v55", "v56", "v57", "v58", "v59", "v60", "v61", "v62", "v63", "v64", "v65", "v66", "v67", "v68", "v69", "v70", "v71", "v72", "v73", "v74", "v75", "v76", "v77", "v78", "v79", "v80", "v81", "v82", "v83", "v84", "v85", "v86", "v87", "v88", "v89", "v90", "v91", "v92", "v93", "v94", "v95", "v100", "v101", "v102", "v103", "v104", "v105", "v106", "v107", "v108", "v109", "v110", "v111", "v124", "v125")

constexpr int H_ACC = 0, H_TAB = 32768, H_STG = H_TAB + NPAD * 8 + 256;
constexpr int F_HP = 4096 + 32, F_BLK = 7 * F_HP, F_TOK = 65536, F_FV = F_TOK + 3 * F_BLK, F_CNT = F_FV + 1296, F_SCR = F_CNT + 64, H_LDS = 163840;
static_assert(H_STG == 133376 && S_END <= H_TAB && H_STG + 16384 <= F_FV && F_SCR + 16 * 512 <= 162000, "LDS map");
typedef __attribute__((ext_vector_type(12))) unsigned u32x12;
DI void dma16(__amdgpu_buffer_rsrc_t rsrc, unsigned lds, unsigned voff, unsigned soff) {
  asm volatile("s_mov_b32 m0, %0\n\ts_nop 0\n\tbuffer_load_dwordx4 %1, %2, %3 offen sc1 lds" :: "s"(lds), "v"(voff), "s"(rsrc), "s"(soff) : "memory");
}
#define GAS __attribute__((address_space(1)))
#define AG_ __ATOMIC_RELAXED, __HIP_MEMORY_SCOPE_AGENT
extern "C" __global__ void __launch_bounds__(1024, 4) k_main(const uint16_t* __restrict__ x, const int* __restrict__ src, const int* __restrict__ dst, const uint16_t* __restrict__ dis,
                                                          const uint16_t* __restrict__ wq, const uint16_t* __restrict__ wk, const uint16_t* __restrict__ wvv, const uint16_t* __restrict__ wo,
                                                          const uint16_t* __restrict__ wr, char* __restrict__ ws, uint16_t* __restrict__ out, int N, int E, int CH) {
#if defined(__HIP_DEVICE_COMPILE__)
  extern __shared__ __attribute__((aligned(16))) char smem[];
  const int t = threadIdx.x, b = blockIdx.x, lane = t & 63, w = __builtin_amdgcn_readfirstlane(t >> 6);
  const int loc = b >> 3, slice = (b & 7) * 4 + (loc >> 3), part = loc & 7;
  auto rsrc = __builtin_amdgcn_make_buffer_rsrc(ws, 0, (int)WS_OVF, 0x00020000);
  if (t == 0) *LW(F_CNT + 56) = (unsigned)__builtin_amdgcn_s_memrealtime();
  asm volatile("" :: "s"(x), "s"(src), "s"(dst), "s"(dis), "s"(wq), "s"(wk), "s"(wvv), "s"(wo), "s"(wr), "s"(ws), "s"(out), "s"(N), "s"(E), "s"(CH));
  STAMP0(0);
  const int mynode = b * NB + lane, job = (loc >> 1) * 8 + (b & 7);
  const bool edge = !(loc & 1), wm = job >= 116, wj = job >= 112; const int qd = job & 3;
  const int e0 = job * CH, e1 = max(e0, min(E, e0 + CH));
  u64 qa = (u64)x, qb = qa; unsigned lim = min(N, 112 * job + 112) * 256, oa = 112 * 256 * job, ob = oa + 16384, sh = 7;
#define SEL(c, v, n) do { auto n_ = (n); asm("" : "+s"(n_)); v = (c) ? n_ : v; } while (0)
  SEL(wj, qa, (u64)wq); SEL(wj, qb, (u64)wk); SEL(wj, lim, 32768u); SEL(wj, oa, (qd >> 1) * 16384u); SEL(wj, ob, (qd & 1) * 16384u);
  SEL(wm, qa, (u64)wvv); SEL(wm, qb, (u64)wo); SEL(wm, ob, (qd & 1) * 128u); SEL(wm, sh, 8u); SEL(job >= 120, lim, 0u);
  SEL(edge, qa, (u64)src); SEL(edge, qb, (u64)dst); SEL(edge, lim, min(e1, E) * 4u); SEL(edge, oa, e0 * 4u); SEL(edge, ob, e0 * 4u); SEL(edge, sh, 7u);
  auto ra = __builtin_amdgcn_make_buffer_rsrc((void*)qa, 0, lim, 0x00020000), rb = __builtin_amdgcn_make_buffer_rsrc((void*)qb, 0, lim, 0x00020000);
  u32x4 pa = __builtin_amdgcn_raw_buffer_load_b128(ra, t * 16 + oa, 0, 0), pb = __builtin_amdgcn_raw_buffer_load_b128(rb, ((t >> 3) << sh) + (t & 7) * 16 + ob, 0, 0);
  const unsigned da = dis[min(112 * job + (t >> 4), N - 1)], db = dis[min(112 * job + 64 + (t >> 4), N - 1)], myd = dis[min(mynode, N - 1)];
  u64 epo = __hip_atomic_load((GAS u64*)(ws + WS_EPO), AG_);
  asm volatile("" ::: "memory");
  unsigned tg[4], tag;
  auto tags = [&](u64 ep) {
    const unsigned lo = __builtin_amdgcn_readfirstlane((unsigned)ep), hi = __builtin_amdgcn_readfirstlane((unsigned)(ep >> 32)), h = ((lo >> 10) + hi * 0x9e3779b9u) * 0x85ebca6bu;
    tg[0] = lo << 22; tg[1] = ~lo << 22; tg[2] = (lo + (h >> 22)) << 22; tg[3] = (3 * lo + (h >> 12)) << 22; tag = lo * 0x9e3779b1u + hi + 0x7f4a7c15u; };
  auto init = [&]() {
    const unsigned f = t < S_S / 16 ? 0u : ~0u; *(u32x4*)(smem + t * 16) = u32x4{f, f, f, f}; *(u32x4*)(smem + 16384 + t * 16) = u32x4{~0u, ~0u, ~0u, ~0u};
    if (t < 76) *LW(t < 64 ? H_TAB + NPAD * 8 + t * 4 : F_CNT + (t - 64) * 4) = 0; };
  if (__builtin_expect(edge, 1)) {
    unsigned* sm = (unsigned*)(smem + H_TAB);
    unsigned* cell = sm;
    unsigned* cnt = sm + 256 * 36;
    u32x4 s4 = pa, d4 = pb;
    if (t < 257) cnt[t] = 0;
    asm volatile("s_waitcnt lgkmcnt(0)\n\ts_barrier" ::: "memory");
    if (w >= 13) {
#pragma unroll
      for (int i = 0; i < 4; ++i) { const int p = (t - 832) + i * 192; const unsigned f = p < 704 ? ~0u : 0u;
        st16(u32x4{f, f, f, f}, rsrc, p < 704 ? (unsigned)WS_SCHED + job * 11264 + p * 16 : p < 720 ? (unsigned)WS_FLAG + job * 256 + (p - 704) * 16 : 0x7fff0000u, 0); } }
    STAMP(0, 1);
    GAS unsigned* ovf = (GAS unsigned*)(ws + WS_OVF) + (size_t)job * CH;
    const unsigned dum = (t & 63) * 36 + 32;
    auto bin = [&](int e) {
      unsigned bk[4], rk[4];
#pragma unroll
      for (int j = 0; j < 4; ++j) { bk[j] = d4[j] / NB; rk[j] = atomicAdd(&sm[e + j < e1 ? 256 * 36 + bk[j] : dum], 1u); }
#pragma unroll
      for (int j = 0; j < 4; ++j) sm[e + j < e1 && rk[j] < 32 ? bk[j] * 36 + (rk[j] & 7) * 4 + (rk[j] >> 3) : dum + 1] = s4[j] | ((d4[j] - bk[j] * NB) << 16);
      if (__builtin_expect(e < e1 && max(max(rk[0], rk[1]), max(rk[2], rk[3])) >= 32, 0))
        _Pragma("nounroll") for (int j = 0; j < 4; ++j) { const unsigned r = j == 0 ? rk[0] : j == 1 ? rk[1] : j == 2 ? rk[2] : rk[3], sv = j == 0 ? s4[0] : j == 1 ? s4[1] : j == 2 ? s4[2] : s4[3],
            dv = j == 0 ? d4[0] : j == 1 ? d4[1] : j == 2 ? d4[2] : d4[3]; if (e + j < e1 && r >= 32) __hip_atomic_store(ovf + atomicAdd(&cnt[256], 1u), sv | (dv << 16), AG_); } };
    if (e0 + 256 * w < e1) bin(e0 + 4 * t);
    if (__builtin_expect(e0 + 4096 < e1, 0)) _Pragma("nounroll") for (int e = e0 + 4096; e < e1; e += 4096) {
      s4 = __builtin_amdgcn_raw_buffer_load_b128(ra, (e + 4 * t) * 4, 0, 0); d4 = __builtin_amdgcn_raw_buffer_load_b128(rb, (e + 4 * t) * 4, 0, 0); bin(e + 4 * t); }
    asm volatile("" : "+v"(epo)); tags(++epo);
    STAMP(0, 2);
    asm volatile("s_waitcnt vmcnt(0) lgkmcnt(0)\n\ts_barrier" ::: "memory");
    STAMP(0, 3);
#pragma unroll
    for (int i = 0; i < 2; ++i) { const int p = t + i * 1024, bk = p >> 3, q = p & 7;
      u32x4 v = *(const u32x4*)(cell + bk * 36 + q * 4); const int n = (int)cnt[bk] - q;
#pragma unroll
      for (int j = 0; j < 4; ++j) v[j] = (n > 8 * j ? v[j] : 0x3fffffu) | tg[j];
      st16(v, rsrc, bk * (NCH * 128) + q * 16, (int)WS_CELL + job * 128); }
    if (t >= 1016) { const unsigned n = cnt[256]; st16(u32x4{n, tag, n, tag}, rsrc, (t - 1016) * 16, (unsigned)WS_OCNT + job * 128); }
    init();
    asm volatile("s_waitcnt lgkmcnt(0)\n\ts_barrier\n\ts_waitcnt vmcnt(0)" ::: "memory");
    __builtin_amdgcn_s_sleep(CW0);
  } else {
    unsigned pfv = 0;
    if (__builtin_expect(!wj, 1)) {
      const u32x4 v[2] = {pa, pb}; const float d[2] = {bf1(da), bf1(db)};
      char* gi = smem + H_TAB;
#pragma unroll
      for (int i = 0; i < 2; ++i) { const int p = t + i * 1024, o = (p & 15) * 1824 + (p >> 4) * 8; unsigned g[4];
#pragma unroll
        for (int c = 0; c < 4; ++c) g[c] = pk(blo(v[i][c]) * d[i], bhi(v[i][c]) * d[i]);
        if (p < 1792) { *(u32x2*)(gi + o) = u32x2{g[0], g[1]}; *(u32x2*)(gi + o + 912) = u32x2{g[2], g[3]}; } }
      STAMP(0, 2);
      asm volatile("s_waitcnt lgkmcnt(0)\n\ts_barrier" ::: "memory");
      STAMP(0, 3);
#pragma unroll
      for (int i = 0; i < 2; ++i) { const int p = t + i * 1024, s = p / 56; st16(*(const u32x4*)(gi + (p + s) * 16), rsrc, p < 1792 ? s * SL + 896 * job + (p - s * 56) * 16 : 0x7fff0000, 0); }
    } else {
      if (job < 116) wprod<0>(pa, pb, wr, rsrc, smem + H_TAB, qd);
      else if (job < 120) wprod<1>(pa, pb, wr, rsrc, smem + H_TAB, qd);
      else if (t < 216) {
        u64 pm; asm volatile("s_getpc_b64 s[62:63]\n\ts_add_u32 s62, s62, k_main@rel32@lo+4\n\ts_addc_u32 s63, s63, k_main@rel32@hi+12" : "={s[62:63]}"(pm) :: "scc");
        pfv = *(const GAS unsigned*)(pm + t * 128); }
    }
    init();
    asm volatile("" : "+v"(epo)); tags(++epo);
    if (WE || !wj) asm volatile("s_waitcnt vmcnt(0)" ::: "memory");
    asm volatile("s_waitcnt lgkmcnt(0)\n\ts_barrier" :: "v"(pfv) : "memory");
    if ((WE || !wj) && t >= 1016) st16(u32x4{tag, ~tag, tag, ~tag}, rsrc, (t - 1016) * 16, (unsigned)WS_RDY + job * 128);
  }
  { const unsigned t0 = __builtin_amdgcn_readfirstlane(*LW(F_CNT + 56)); while ((unsigned)__builtin_amdgcn_s_memrealtime() - t0 < CW1) __builtin_amdgcn_s_sleep(2); }
  STAMP(0, 4); FLUSH(0);
  STAMP(1, 0);
  auto cells = [&](int bk, int tt) {
    u32x4 v; bool bad = true;
    _Pragma("nounroll") for (int sp = 0; sp < (1 << 20); ++sp) {
      if (bad) { v = __builtin_amdgcn_raw_buffer_load_b128(rsrc, (unsigned)WS_CELL + bk * (NCH * 128) + tt * 16, 0, 16);
        bad = (((v[0] ^ tg[0]) | (v[1] ^ tg[1]) | (v[2] ^ tg[2]) | (v[3] ^ tg[3])) >> 22) != 0; }
      if (__builtin_expect(!__builtin_amdgcn_ballot_w64(bad), 1)) break;
      __builtin_amdgcn_s_sleep(CWR);
    }
    return v | 0xffc00000u; };
  auto ovfn = [&](int c) { u64 v = 0;
    _Pragma("nounroll") for (int sp = 0; sp < (1 << 20); ++sp) { v = __hip_atomic_load((GAS u64*)(ws + WS_OCNT + c * 128), AG_); if ((unsigned)(v >> 32) == tag) break; __builtin_amdgcn_s_sleep(4); }
    return min((unsigned)v, (unsigned)CH); };
  auto ready = [&](int j0, int n) {
    const u64 want = tag | (u64)~tag << 32;
    _Pragma("nounroll") for (int sp = 0; sp < (1 << 20); ++sp) {
      const u64 a = __hip_atomic_load((GAS u64*)(ws + WS_RDY + (j0 + min(lane, n - 1)) * 128), AG_), c = __hip_atomic_load((GAS u64*)(ws + WS_RDY + (j0 + min(lane + 64, n - 1)) * 128), AG_);
      if (!__builtin_amdgcn_ballot_w64(a != want || c != want)) break;
      __builtin_amdgcn_s_sleep(4); }
    asm volatile("" ::: "memory"); };
  const u32x4 ev = cells(b, t);
  asm volatile("s_waitcnt vmcnt(0)" ::: "memory");
  if (t == 0) *(LAS u64*)(size_t)(F_CNT + 48) = epo;
  const unsigned ownoff = w * 6272 + part * 784 + lane * 16;
  const int mko = lane < NB ? ownoff : 0x7fff0000;
#define MARK(o, k, d) st16(u32x4{~0u, ~0u, ~0u, ~0u}, rsrc, (o), (k) * HS + slice * SL + (d))
  const unsigned wb = (unsigned)WS_W + slice * WSZ;
#define MARK4(k) do { if (w < 4) { MARK(mko, k, 0); MARK(mko, k, 4 * 6272); MARK(mko, k, 8 * 6272); MARK(mko, k, 12 * 6272); } } while (0)
  const unsigned alo = lane < 56 ? part * 12544 + lane * 16 : 0x7fff0000;
#define MARKA4(k) do { if (w < 4) { MARK(alo, k, w * 896); MARK(alo, k, (w + 4) * 896); MARK(alo, k, (w + 8) * 896); if (w < 2) MARK(alo, k, (w + 12) * 896); } } while (0)
#define MARKA7(k) do { MARK(alo, k, (w - 9) * 896); MARK(alo, k, (w - 2) * 896); } while (0)
  unsigned tch = 0;
  STAMP(1, 1);
  unsigned e[3] = {ev[0], ev[1], ev[2]}, rk[3], na[3], tq[6], ts[2], ex[2] = {ev[3], ~0u}, rx[2] = {0, 0}; u64 vm[3], tm[3];
  PASS1(rk, na, vm, tq[0], e);
  if (__builtin_expect(ex[0] != ~0u, 0)) { asm volatile("s_waitcnt lgkmcnt(0)" ::: "memory"); rx[0] = l_add(S_DEG + (ex[0] >> 16 & 63) * 4, 1u);
    if ((t & 7) == 7) *LW(S_FULL + l_add(S_CNT + 4, 1u) * 4) = t >> 3; }
  MARK4(1);
  f32x4 wv[8];
  auto wget = [&]() {
    if (w == 9 || w == 10) { ready(0, 112);
      asm volatile("buffer_load_dword %0, %1, %2, 0 offen sc1" : "+v"(tch) : "v"(lane < NB ? slice * SL + (part * 98 + (w - 9) * NB + lane) * 128 : 0x7fff0000), "s"(rsrc) : "memory");
      if (lane == 0) l_add(S_CNT + 20, 1u); }
    if (w >= 12 || (w == 11 && part == 0)) { __builtin_amdgcn_s_sleep(WSL); ready(112, b == 0 ? 16 : 8); }
    if (w >= 12) wfetch(wv, rsrc, part * 4 + w - 12, lane); else if (w == 11 && part == 0) ffetch(wv, rsrc, wr, lane); };
  STAMP(1, 2);
  asm volatile("s_waitcnt lgkmcnt(0)\n\ts_barrier" : "+v"(rk[0]), "+v"(rk[1]), "+v"(rk[2]) :: "memory");
  if (!WE && !edge && wj && t >= 1016) st16(u32x4{tag, ~tag, tag, ~tag}, rsrc, (t - 1016) * 16, (unsigned)WS_RDY + job * 128);
  STAMP(1, 3);
  unsigned nf = *LW(S_CNT + 4); int dg = *LW(S_DEG + lane * 4);
  asm volatile("" : "+v"(nf), "+v"(dg));
  const unsigned nfull = __builtin_amdgcn_readfirstlane(nf);
  bool generic = false;
  if (__builtin_expect(nfull != 0, 0)) {
    for (unsigned f = 0; f < nfull; ++f) { const unsigned c = *LW(S_FULL + f * 4), n = ovfn(c);
      GAS unsigned* ovf = (GAS unsigned*)(ws + WS_OVF) + (size_t)c * CH;
      for (unsigned i = t; i < n; i += 1024) { const unsigned v = __hip_atomic_load(ovf + i, AG_), dl = (v >> 16) - b * NB;
        if (dl < (unsigned)NB) { const unsigned p = l_add(S_CNT + 8, 1u); if (p < 1024) *LW(S_EXTRA + p * 4) = (v & 0xffffu) | (dl << 16); } } }
    __syncthreads();
    const unsigned nx = *LW(S_CNT + 8);
    generic = nx > 1024;
    if ((unsigned)t < nx) { ex[1] = *LW(S_EXTRA + t * 4); rx[1] = l_add(S_DEG + (ex[1] >> 16) * 4, 1u); }
    __syncthreads();
    dg = *LW(S_DEG + lane * 4);
  }
  int T, ns, tc; u64 smask;
#define TSTEP(B) "s_add_u32 %2, %0, " B "\n\tv_cmp_le_i32 vcc, %2, %4\n\ts_bcnt1_i32_b64 %1, vcc\n\ts_cmp_gt_u32 %1, 15\n\ts_cselect_b32 %0, %2, %0\n\t"
  asm volatile("s_mov_b32 %0, 25\n\t" TSTEP("8") TSTEP("4") TSTEP("2") TSTEP("1") "s_sub_u32 %0, %0, 1\n\t"
               ".Lt_%=:\n\ts_add_u32 %0, %0, 1\n\ts_lshl_b32 %2, %0, 1\n\tv_cmp_lt_i32 %3, %0, %4\n\tv_cmp_lt_i32 vcc, %2, %4\n\ts_bcnt1_i32_b64 %1, %3\n\ts_bcnt1_i32_b64 %2, vcc\n\t"
               "s_lshl4_add_u32 %2, %2, %1\n\ts_cmp_gt_u32 %0, 40\n\ts_cselect_b32 %2, 0, %2\n\ts_cmp_gt_u32 %2, 15\n\ts_cbranch_scc1 .Lt_%="
               : "=&s"(T), "=&s"(ns), "=&s"(tc), "=&s"(smask) : "v"(dg) : "scc", "vcc");
  generic = generic || T > TS;
  if (generic) { MARKA4(2); MARKA4(3); MARKA4(4); MARKA4(5); }
  if (__builtin_expect(!generic, 1)) {
  const bool split = dg > T;
  const int nbelow = mbc(smask);
  const int base = split ? 2 * nbelow : 2 * ns + lane - nbelow;
  const int info = base * 4 + split;
  STAMP(1, 4);
  PASS2(e, rk, na, tq, tm, ts, vm, info, lane * 4);
  if (__builtin_expect((ex[0] & ex[1]) != ~0u, 0))
    for (int j = 0; j < 2; ++j) { const unsigned ee = j ? ex[1] : ex[0], rr = j ? rx[1] : rx[0];
      if (ee != ~0u) { const unsigned inf = *LW(1280 + (ee >> 16 & 63) * 4); *LW(S_LEFT + l_add(S_CNT, 1u) * 4) = (ee & 0xffffu) | ((inf & ~3u) + (inf & rr & 1) * 4) << 16; } }
  if (w == 15) {
    const int tl = lane < NB ? base : lane - NB < ns ? 2 * (lane - NB) + 1 : lane; const float d = bf1(myd);
    __builtin_amdgcn_raw_buffer_store_b64(u32x2{(lane < NB ? ((b & 1) * NB + lane) * 8u : 800u) | (tl < 2 * ns ? 0x80000000u : 0u), mynode < N ? min(__float_as_uint(d * d), 0xfffffffeu) : 0u}, rsrc, tl * 8, (int)WS_META + b * 512, 16);
  }
  MARKA4(2);
  STAMP(1, 5);
  asm volatile("s_waitcnt lgkmcnt(0)\n\ts_barrier" ::: "memory");
  STAMP(1, 6);
  { const unsigned hsh = ((t + b * 1024) * 2654435761u) >> 7;
    unsigned lv = *LW(S_LEFT + t * 4), l2 = *LW(S_LEFT + 4096 + t * 4);
    const bool more = __builtin_amdgcn_ballot_w64(l2 != ~0u) != 0;
    for (int it = 1;; ++it) {
      PASS3(tm[0], tm[1], tm[2], lv, hsh);
      if (__builtin_expect(!more || it == 5, 1)) break;
      lv = *LW(S_LEFT + 4096 * it + t * 4);
    } }
  STAMP(1, 7);
  asm volatile("s_waitcnt lgkmcnt(0)" ::: "memory");
  if (lane == 0) l_add(S_CNT + 16, 1u);
  if (w >= 9 && (MO || w < 11)) MARKA7(3);
  if (w < 5) lspin(S_CNT + 16, 16);
  STAMP(1, 8);
  if (w < 5) {
    const unsigned C = *LW(S_COL + lane * 4 + (w >> 2) * 256) >> (8 * (w & 3));
    u32x4 v = *(LAS u32x4*)(size_t)(S_S + w * 16 + lane * 80);
#pragma unroll
    for (int j = 0; j < 4; ++j) { const u64 m0 = __builtin_amdgcn_ballot_w64((C & (1u << (2 * j))) == 0), m1 = __builtin_amdgcn_ballot_w64((C & (2u << (2 * j))) == 0);
      const unsigned fl = __builtin_ctz((unsigned)m0 | 0x80000000u) | __builtin_ctz((unsigned)m1 | 0x80000000u) << 16;
      const unsigned fh = __builtin_ctz((unsigned)(m0 >> 32) | 0x80000000u) | __builtin_ctz((unsigned)(m1 >> 32) | 0x80000000u) << 16;
      const unsigned pad = NPAD * 0x10001u + (lane < 32 ? fl : fh), x = v[j];
      asm("v_pk_min_u16 %0, %1, %2" : "=v"(v[j]) : "v"(x), "v"(pad)); }
    st16(v, rsrc, lane * 16, (int)WS_SCHED + b * 5120 + w * 1024);
  }
  }
  STAMP(1, 9);
  GAS unsigned* fg = (GAS unsigned*)(ws + WS_FLAG);
  if (w >= 11) __builtin_amdgcn_s_setprio(3);
  wget();
  if (!MO && w >= 11 && !generic) MARKA7(3);
  if (w >= 12) wfinish(wv, rsrc, wb, part * 4 + w - 12, lane, H_STG + (t >> 6) * 1024); else if (w == 11 && part == 0) ffinish(wv, rsrc, wb, lane);
  if (w >= 11) __builtin_amdgcn_s_setprio(0);
  if (b == 0 && w == 15) { lspin(S_CNT + 20, 2); if (lane == 0) __hip_atomic_store((GAS u64*)(ws + WS_EPO), epo, AG_); }
  if (w < 5) {
    if (lane == 0 && l_add(S_CNT + 12, 1u) == 4) __hip_atomic_store(fg + b * 32, generic ? 3u : 1u, __ATOMIC_RELAXED, __HIP_MEMORY_SCOPE_AGENT);
  } else if (w != 8 && w < 12) {
    const int p0 = w < 8 ? w - 5 : w - 6;
    lspin(S_CNT + 20, 2); lspin(S_CNT + 12, 5); __builtin_amdgcn_s_sleep(2);
    __builtin_amdgcn_s_setprio(2);
#pragma unroll
    for (int i = 0; i < 17; ++i) if (i < 16 || p0 < 2) dma16(rsrc, H_TAB + (p0 + 6 * i) * 1024, lane * 16, slice * SL + (p0 + 6 * i) * 1024);
    __builtin_amdgcn_s_setprio(0);
  }
  if (w == 8) {
    GAS unsigned* mine = fg + (((lane & 31) >> 1) * 16 + part * 2 + (lane & 1)) * 32;
    unsigned f = lane < 32 ? 0 : 1;
    lspin(S_CNT + 12, 5);
    __builtin_amdgcn_s_setprio(3);
    __builtin_amdgcn_s_sleep(POLL0);
    _Pragma("nounroll") for (int sp = 0; sp < (1 << 20); ++sp) {
      if (f == 0) f = __hip_atomic_load(mine, __ATOMIC_RELAXED, __HIP_MEMORY_SCOPE_AGENT);
      if (__builtin_amdgcn_ballot_w64(f & 2)) break;
      const u64 m = __builtin_amdgcn_ballot_w64(f != 0);
      __hip_atomic_store(LW(F_CNT + 16), (unsigned)m, WG_);
      if (m == ~0ull) break;
      __builtin_amdgcn_s_sleep(POLLS);
    }
    if (__builtin_amdgcn_ballot_w64(f == 0 || (f & 2))) __hip_atomic_store(LW(F_CNT + 32), 1u, WG_);
    __builtin_amdgcn_s_setprio(0);
  }
  asm volatile("s_waitcnt vmcnt(0) lgkmcnt(0)" : "+v"(tch) :: "memory");
  if (lane == 0) l_add(F_CNT + 36, 1u);
  unsigned slow = 0;
  const int xt = min(12288 + b * 4, NPAD - 4);
  unsigned pp = (b * 48 / 98 & 7) | ((b * 48 + 47) / 98 & 7) << 4 | (xt / 98 & 7) << 8 | ((xt + 3) / 98 & 7) << 12, t7 = 7 * HS + b * 384 + (w - 13) * 128;
  asm volatile("" : "+s"(pp), "+s"(t7));
  const int bk0 = w * 16 + part * 2;
  const unsigned fo = (unsigned)WS_FLAG + b * 128, flow = __builtin_amdgcn_readfirstlane(generic ? 3 : 1);
  STAMP(1, 10);
  u32x6 O;
#pragma unroll
  for (int i = 0; i < 6; ++i) { const int c = lane + 64 * i; O[i] = w * 6272 + (c + (c >= NB * part ? NB : 0)) * 16; }
  {
    u32x8 M; u64 mk0, mk1;
    M[0] = (lane & 3) == 0 ? 0x3f80u : (lane & 3) == 1 ? 0x3f800000u : 0u; M[1] = (lane & 3) == 2 ? 0x3f80u : (lane & 3) == 3 ? 0x3f800000u : 0u;
    M[2] = M[3] = M[4] = M[5] = ownoff; M[6] = H_STG + w * 1024 + lane * 16; M[7] = ownoff;
    const int grp = w >> 3, dly = (w >= 12 ? DLYC : grp ? DLYB : w >= 4 ? DLYA1 : DLYA) | (w == 0 ? 0x40 : w < 8 ? 0 : w < 10 ? 0x7c : w < 14 ? 0x40 : 0) << 24 | ((MKB ? w >= 9 : w < 7) ? 0x1e : 0) << 24 | ((MB ? grp : !grp) ? 0x20 : 0) << 24;
    unsigned soff = slice * SL, hop = 0, tmp, retry = 0, role = grp, dcur = grp ? DLY0B : DLY0A, l16 = lane * 16; const unsigned hs = HS; const u64 m49 = (1ull << NB) - 1;
    const unsigned so = (unsigned)WS_SCHED + bk0 * 5120, mo = (unsigned)WS_META + bk0 * 512, sb = H_STG + w * 1024, ms = wb + (w & 7) * 4096 - sb, fh = w ? 99 : 6, ph = !PFON ? 99 : (unsigned)(0xff666622ffffffffull >> (4 * w)) & 15, pm = w < 10 ? HS : 0;
    const u64 mt = (MKB ? w < 9 : w >= 7) ? 0 : ML == 0 ? 0x56780ull : ML == 1 ? 0x0000077000056660ull : 0x0005555000044440ull;
    const int pbk = (w & 1) * 2 + (lane >> 5);
    const unsigned mo2 = alo + (MKB ? w - 9 : w) * 896;
    const unsigned po = w >= 10 ? wb + 32768 + ((w - 10) * 64 + lane) * 128 : (lane & 31) * SL + (pbk < 3 ? (b * 3 + pbk) * 128 : min(12288 + b * 4, NPAD - 4) * 8);
    const int fp = 0;
    const unsigned bits = 3u << (2 * w);
    if (AP && w < 8) __builtin_amdgcn_s_setprio(1);
    HOP_LOOP(soff, hop, tmp, retry, mk0, mk1, role, dcur, rsrc, grp, dly, hs, m49, so, mo, sb, fp, ms, fo, fh, flow, ph, pm, mt, bits, M, O, l16, po, mo2);
    slow = hop >> 4;
  }
  if (__builtin_expect(slow, 0)) {
    asm volatile("s_waitcnt vmcnt(0)" ::: "memory");
    __syncthreads();
    unsigned z1 = 0; asm volatile("" : "+v"(z1));
    const int lane = __builtin_amdgcn_mbcnt_hi(~0u, __builtin_amdgcn_mbcnt_lo(~0u, z1)), tid = w * 64 + lane, t = tid;
    const unsigned ownoff = w * 6272 + part * 784 + lane * 16;
    tags(*(volatile LAS u64*)(size_t)(F_CNT + 48));
    float* acc = (float*)(smem + H_ACC);
    const int n0 = w * 784 + part * 98 + 2 * lane;
    float d0 = (lane < NB && n0 < N) ? bf1(dis[n0]) : 0.f, d1 = (lane < NB && n0 + 1 < N) ? bf1(dis[n0 + 1]) : 0.f;
    d0 *= d0; d1 *= d1;
    auto add = [&](unsigned s, int li) { const u32x2 v = *(const u32x2*)(smem + H_TAB + s * 8); float* a = acc + li * 4;
      atomicAdd(a, blo(v[0])); atomicAdd(a + 1, bhi(v[0])); atomicAdd(a + 2, blo(v[1])); atomicAdd(a + 3, bhi(v[1])); };
    const unsigned alo = lane < 56 ? part * 12544 + lane * 16 : 0x7fff0000;
    MARKA4(6); MARKA4(7); if (ML) { MARKA4(4); MARKA4(5); }
    if (t == 0) __hip_atomic_store((GAS unsigned*)(ws + fo), flow | 0x100u, __ATOMIC_RELAXED, __HIP_MEMORY_SCOPE_AGENT);
    if (w == 0) for (int sp = 0; sp < (1 << 20); ++sp) {
      if (!__any(__hip_atomic_load((GAS unsigned*)(ws + (fo & ~0x1c00u) + (lane & 7) * 1024), __ATOMIC_RELAXED, __HIP_MEMORY_SCOPE_AGENT) < 0x100u)) break;
      __builtin_amdgcn_s_sleep(8); }
    for (int hop = 0; hop < 7; ++hop) {
      const int so = (hop + 1) * HS + slice * SL;
      for (int i = tid; i < 1568 * 4; i += 1024) acc[i] = 0.f;
      __syncthreads();
      for (int i = 0; i < 32; ++i) { const int bk = (i >> 1) * 16 + part * 2 + (i & 1);
        const u32x4 c4 = cells(bk, tid);
        for (int j = 0; j < 4; ++j) if (c4[j] != ~0u) add(c4[j] & 0xffffu, i * NB + (c4[j] >> 16 & 63)); }
      for (int c = 0; c < NCH; ++c) { const unsigned n = ovfn(c); GAS unsigned* ovf = (GAS unsigned*)(ws + WS_OVF) + (size_t)c * CH;
        for (unsigned i = tid; i < n; i += 1024) { const unsigned v = __hip_atomic_load(ovf + i, AG_), nd = v >> 16, ww = nd / 784, r = nd - ww * 784 - part * 98; if (r < 98u) add(v & 0xffffu, ww * 98 + r); } }
      __syncthreads();
      u32x4 own = {0, 0, 0, 0};
      if (lane < NB) { const f32x4 a = *(const f32x4*)(acc + (w * 98 + 2 * lane) * 4) * d0, c = *(const f32x4*)(acc + (w * 98 + 2 * lane + 1) * 4) * d1;
        own = u32x4{min(pk(a[0], a[1]), 0xfffffffeu), min(pk(a[2], a[3]), 0xfffffffeu), min(pk(c[0], c[1]), 0xfffffffeu), min(pk(c[2], c[3]), 0xfffffffeu)};
        asm volatile("s_waitcnt vmcnt(0)" ::: "memory");
        st16(own, rsrc, ownoff, so);
        *(u32x4*)(smem + H_TAB + ownoff) = own; }
      if (hop < 6) for (int i = 0; i < 6; ++i) if (i < 5 || lane < 23) {
        const int c = lane + 64 * i; const unsigned off = w * 6272 + (c + (c >= NB * part ? NB : 0)) * 16; u32x4 v;
        _Pragma("nounroll") for (int sp = 0; sp < (1 << 22); ++sp) { v = __builtin_amdgcn_raw_buffer_load_b128(rsrc, off, so, 16);
          if (max(max(v[0], v[1]), max(v[2], v[3])) != ~0u) break; __builtin_amdgcn_s_sleep(8); }
        *(u32x4*)(smem + H_TAB + off) = v; }
      asm volatile("s_waitcnt vmcnt(0)" ::: "memory");
      __syncthreads();
    }
    if (t == 0) __hip_atomic_store((GAS unsigned*)(ws + fo), flow | 0x600u, __ATOMIC_RELAXED, __HIP_MEMORY_SCOPE_AGENT);
    dma16(rsrc, w * 2048, lane * 16, wb + w * 2048); dma16(rsrc, w * 2048 + 1024, lane * 16, wb + w * 2048 + 1024);
    if (w == 0 || (w == 1 && lane < 17)) dma16(rsrc, F_FV + w * 1024, lane * 16, wb + 65536 + w * 1024);
    asm volatile("s_waitcnt vmcnt(0)" ::: "memory");
  }
  {
    { unsigned u1, u2; const unsigned fb = (unsigned)WS_FLAG; PRE_FINAL(u1, u2, w, rsrc, fb, pp, t7); }
    unsigned z0 = 0; asm volatile("" : "+v"(z0));
    const int ln = __builtin_amdgcn_mbcnt_hi(~0u, __builtin_amdgcn_mbcnt_lo(~0u, z0));
    const int r = ln & 31, h = ln >> 5, nd = r & 3, hp = r >> 2;
    auto rsx = __builtin_amdgcn_make_buffer_rsrc((void*)x, 0, N * 256, 0x00020000);
    auto rso = __builtin_amdgcn_make_buffer_rsrc((void*)out, 0, N * 256, 0x00020000);
    const int node0 = w < 12 ? b * 48 + w * 4 : xt, node = node0 + nd;
    const int act = N - (w < 12 ? node0 : 12288 + b * 4), qe = w < 12 ? 0 : w - 12;
    u32x12 C;
    C[0] = node * 256 + h * 16 + qe * 64; C[1] = (ln >> 3) * SL + (ln & 7) * 16; C[2] = hp * HS + (8 * qe + 2 * h) * SL + node * 8; C[3] = C[2];
    C[4] = F_TOK + (w >> 2) * F_BLK + (hp - 1) * F_HP + h * 256 + ((w & 3) * 4 + nd) * 8;
    C[5] = min(node, N - 1) * 2; C[6] = F_FV + ((((ln >> 4) ^ (ln >> 2)) & 1) ? 1024 : (ln & 3) * 256) + h * 16 + qe * 64; C[7] = w == 0 ? r * SL + (b * 3 + h) * 128 : (b * 48 + 32) * 256 + ln * 64; C[8] = w == 0 ? r * SL + (h ? xt * 8 : (b * 3 + 2) * 128) : xt * 256 + (ln & 7) * 128; C[9] = ln * 16;
    C[10] = F_SCR + w * 512 + r * 16; C[11] = F_SCR + w * 512 + (nd + 4 * h) * 16;
    int ooff = act <= 0 || (w >= 12 && (ln & 12)) ? 0x7fff0000 : node * 256 + 16 * h + (w < 12 ? 32 * hp : qe * 64 + (ln & 16) * 2); asm volatile("" : "+v"(ooff));
    const unsigned tm0 = F_TOK + (w >> 2) * F_BLK + (w & 3) * 1024, tso = HS + (w & 3) * 8 * SL + b * 384 + (w >> 2) * 128, wso = wb;
    u32x4 res; unsigned t1, swr; u64 t2;
    const int fq = 0;
    FINAL_ASM(res, C, t1, t2, swr, w, rsrc, rsx, dis, tm0, tso, wso, fo, flow, t7, act, fq);
    st16(res, rso, ooff, 0);
  }
#endif
}

extern "C" void kernel_launch(void* const* d_in, const int* in_sizes, int n_in, void* d_out, int out_size, void* d_ws, size_t ws_size, hipStream_t stream) {
  const uint16_t* x = (const uint16_t*)d_in[0];
  const int* src = (const int*)d_in[1];
  const int* dst = (const int*)d_in[2];
  const uint16_t* dis = (const uint16_t*)d_in[3];
  const int N = in_sizes[3], E = in_sizes[1];
  const int CH = ((E + NCH - 1) / NCH + 3) & ~3;
  char* ws = (char*)d_ws + (WSEND ? (ws_size / WSEND - (WS_OVF + (size_t)NCH * CH * 4)) & ~(size_t)0xffff : 0);
  (void)hipFuncSetAttribute((const void*)k_main, hipFuncAttributeMaxDynamicSharedMemorySize, H_LDS);
  k_main<<<256, 1024, H_LDS, stream>>>(x, src, dst, dis, (const uint16_t*)d_in[4], (const uint16_t*)d_in[5], (const uint16_t*)d_in[6], (const uint16_t*)d_in[7], (const uint16_t*)d_in[8], ws, (uint16_t*)d_out, N, E, CH);
}
```
